```python
import torch

def _softmax(x, axis=-1):
    return torch.softmax(x, dim=axis)

def _gelu(x):
    return torch.nn.functional.gelu(x, approximate='tanh')

def _silu(x):
    return torch.nn.functional.silu(x)

def _sigmoid(x):
    return torch.sigmoid(x)

def _layernorm(x, axis=-1, eps=1e-05):
    m = x.mean(dim=axis, keepdim=True)
    v = x.var(dim=axis, keepdim=True, unbiased=False)
    return (x - m) / torch.sqrt(v + eps)

def _scatter_add(tgt, idx, src, axis=0):
    return tgt.clone().index_add_(axis, idx, src)

def _segment_sum(src, seg, n):
    out = torch.zeros((n,) + src.shape[1:], dtype=src.dtype)
    return out.index_add_(0, seg, src)

def _pad(x, pw):
    flat = tuple((v for pair in reversed(pw) for v in pair))
    return torch.nn.functional.pad(x, flat)
kernel_name = 'interleave_layers__memory_aug_recurrent_mem_tr__dense_activation_silu__prompt'

def setup_inputs():

    _w = lambda a: (lambda t: t.to(torch.bfloat16) if t.is_floating_point() else t)(torch.as_tensor(a))

    def _A_setup():
        B, S, Dm = (1, 2048, 640)
        H, Dh = (32, 128)
        M, seg = (8, 32)
        seg = min(seg, S)
        S = S // seg * seg
        return {'x': _w((lambda *s: torch.randn(*s, dtype=torch.bfloat16) * 0.05)(B, S, Dm)), 'mem0': _w((lambda *s: torch.randn(*s, dtype=torch.bfloat16) * 0.4)(B, M, Dm)), 'Wq': _w((lambda *s: torch.randn(*s, dtype=torch.bfloat16) * 1.6)(Dm, H, Dh) * Dm ** (-0.5)), 'Wk': _w((lambda *s: torch.randn(*s, dtype=torch.bfloat16) * 1.6)(Dm, H, Dh) * Dm ** (-0.5)), 'Wv': _w((lambda *s: torch.randn(*s, dtype=torch.bfloat16) * 0.4)(Dm, H, Dh) * Dm ** (-0.5)), 'Wo': _w((lambda *s: torch.randn(*s, dtype=torch.bfloat16) * 0.4)(H, Dh, Dm) * (H * Dh) ** (-0.5)), 'seg_len': seg}
    _A_in = _A_setup()

    def _B_setup():
        B, S, D, F = (1, 2048, 640, 1728)
        return {'x': _w((lambda *s: torch.randn(*s, dtype=torch.bfloat16) * 0.05)(B, S, D)), 'w1': _w((lambda *s: torch.randn(*s, dtype=torch.bfloat16) * 1.6)(D, F) * D ** (-0.5)), 'w2': _w((lambda *s: torch.randn(*s, dtype=torch.bfloat16) * 0.2)(F, D) * F ** (-0.5))}
    _B_in = _B_setup()
    return {'A_x': _A_in['x'], 'A_mem0': _A_in['mem0'], 'A_Wq': _A_in['Wq'], 'A_Wk': _A_in['Wk'], 'A_Wv': _A_in['Wv'], 'A_Wo': _A_in['Wo'], 'A_seg_len': _A_in['seg_len'], 'B_x': _B_in['x'], 'B_w1': _B_in['w1'], 'B_w2': _B_in['w2']}

def reference(A_x, A_mem0, A_Wq, A_Wk, A_Wv, A_Wo, A_seg_len, B_x, B_w1, B_w2):

    def _A_ref(x, mem0, Wq, Wk, Wv, Wo, seg_len):
        B, S, Dm = x.shape
        H, Dh = (Wq.shape[1], Wq.shape[2])
        M = mem0.shape[1]
        scale = Dh ** (-0.5)
        n_seg = S // seg_len
        L = M + seg_len
        idx = torch.arange(L)
        mask = (idx[:, None] < M) | (idx[:, None] >= idx[None, :])
        mem = mem0
        outs = []
        for s in range(n_seg):
            xs = x[:, s * seg_len:(s + 1) * seg_len, :].clone()
            h = torch.concatenate([mem, xs], axis=1)
            q = torch.einsum('btd,dhe->bhte', h, Wq)
            k = torch.einsum('btd,dhe->bhte', h, Wk)
            v = torch.einsum('btd,dhe->bhte', h, Wv)
            scores = torch.einsum('bhqe,bhke->bhqk', q, k) * scale
            scores = torch.where(mask[None, None], scores, -1000000000.0)
            p = _softmax(scores, axis=-1)
            o = torch.einsum('bhqk,bhke->bhqe', p, v)
            o = torch.einsum('bhte,hed->btd', o, Wo)
            mem = o[:, :M, :].clone()
            outs.append(o[:, M:, :])
        return torch.concatenate(outs, axis=1)

    def _B_ref(x, w1, w2):
        h = torch.einsum('bsd,df->bsf', x, w1)
        h = _silu(h)
        out = torch.einsum('bsf,fd->bsd', h, w2)
        return out
    x = A_x
    x = x + _A_ref(_layernorm(x, axis=-1), A_mem0, A_Wq, A_Wk, A_Wv, A_Wo, A_seg_len)
    x = x + _B_ref(_layernorm(x, axis=-1), B_w1, B_w2)
    x = x + _A_ref(_layernorm(x, axis=-1), A_mem0, A_Wq, A_Wk, A_Wv, A_Wo, A_seg_len)
    x = x + _B_ref(_layernorm(x, axis=-1), B_w1, B_w2)
    return x
```

```cpp
#include <hip/hip_runtime.h>
#include <hip/hip_bf16.h>
#include <stdint.h>

typedef unsigned short u16;
typedef __attribute__((ext_vector_type(8))) short bf16x8;
typedef __attribute__((ext_vector_type(4))) float f32x4;

__device__ __forceinline__ float bf2f(u16 v) { return __uint_as_float(((unsigned)v) << 16); }
__device__ __forceinline__ u16 f2bf(float f) { __hip_bfloat16 h = __float2bfloat16(f); return *(u16*)&h; }
__device__ __forceinline__ float rbf(float f) { return bf2f(f2bf(f)); }

constexpr int S = 2048, D = 640, H = 32, DH = 128, HD = 4096, M = 8, SEG = 32, NSEG = 64, F = 1728;

__global__ void ln_kernel(const u16* x, u16* y) {
    int row = blockIdx.x; int lane = threadIdx.x;
    float v[10]; float s = 0;
    for (int i = 0; i < 10; ++i) { v[i] = bf2f(x[row * D + lane + 64 * i]); s += v[i]; }
    for (int m = 32; m >= 1; m >>= 1) s += __shfl_xor(s, m);
    float mean = s / D; float q = 0;
    for (int i = 0; i < 10; ++i) { float d = v[i] - mean; q += d * d; }
    for (int m = 32; m >= 1; m >>= 1) q += __shfl_xor(q, m);
    float var = q / D;
    float r = 1.0f / sqrtf(var + 1e-5f);
    for (int i = 0; i < 10; ++i) y[row * D + lane + 64 * i] = f2bf((v[i] - mean) * r);
}

template <int EPI>
__global__ void __launch_bounds__(256) gemm_kernel(const u16* A, int lda, const u16* B, int ldb, u16* C, int ldc, const u16* R, int Mrows, int N, int K) {
    __shared__ u16 As[64][40];
    __shared__ u16 Bs[64][40];
    int bm = blockIdx.y * 64, bn = blockIdx.x * 64;
    int tid = threadIdx.x, wave = tid >> 6, lane = tid & 63;
    int wr = wave >> 1, wc = wave & 1;
    f32x4 acc[2][2] = {};
    for (int k0 = 0; k0 < K; k0 += 32) {
        { int r = tid >> 2, c = (tid & 3) * 8;
          bf16x8 v = {};
          if (bm + r < Mrows) v = *(const bf16x8*)&A[(size_t)(bm + r) * lda + k0 + c];
          for (int j = 0; j < 8; ++j) As[r][c + j] = v[j]; }
        { int kk = tid >> 3, c = (tid & 7) * 8;
          bf16x8 v = *(const bf16x8*)&B[(size_t)(k0 + kk) * ldb + bn + c];
          for (int j = 0; j < 8; ++j) Bs[c + j][kk] = v[j]; }
        __syncthreads();
        bf16x8 a[2], b[2];
        for (int i = 0; i < 2; ++i) { for (int j = 0; j < 8; ++j) { a[i][j] = As[wr * 32 + i * 16 + (lane & 15)][(lane >> 4) * 8 + j]; b[i][j] = Bs[wc * 32 + i * 16 + (lane & 15)][(lane >> 4) * 8 + j]; } }
        for (int i = 0; i < 2; ++i) for (int j = 0; j < 2; ++j)
            acc[i][j] = __builtin_amdgcn_mfma_f32_16x16x32_bf16(__builtin_bit_cast(__attribute__((ext_vector_type(8))) __bf16, a[i]), __builtin_bit_cast(__attribute__((ext_vector_type(8))) __bf16, b[j]), acc[i][j], 0, 0, 0);
        __syncthreads();
    }
    for (int i = 0; i < 2; ++i) for (int j = 0; j < 2; ++j) for (int r = 0; r < 4; ++r) {
        int row = bm + wr * 32 + i * 16 + (lane >> 4) * 4 + r, col = bn + wc * 32 + j * 16 + (lane & 15);
        if (row < Mrows) {
            float v = acc[i][j][r];
            if (EPI == 1) { v = rbf(v); v = v / (1.0f + __expf(-v)); }
            if (EPI == 2) { v = rbf(v) + bf2f(R[(size_t)row * ldc + col]); }
            C[(size_t)row * ldc + col] = f2bf(v);
        }
    }
}

template <bool MEMROWS>
__global__ void attn_kernel(const u16* qkv_x, const u16* qkv_m, u16* out, int seg0) {
    int h = blockIdx.x; int seg = seg0 + blockIdx.y;
    __shared__ float ks[40][129], vs[40][129], qs[32][129], ps[32][41];
    int tid = threadIdx.x;
    const u16* mb = qkv_m + (size_t)seg * 8 * 3 * HD;
    const u16* xb = qkv_x + (size_t)seg * 32 * 3 * HD;
    for (int r = 0; r < 40; ++r) {
        const u16* p = r < 8 ? mb + (size_t)r * 3 * HD : xb + (size_t)(r - 8) * 3 * HD;
        ks[r][tid] = bf2f(p[HD + h * DH + tid]); vs[r][tid] = bf2f(p[2 * HD + h * DH + tid]);
    }
    const int NR = MEMROWS ? 8 : 32;
    for (int r = 0; r < NR; ++r) { const u16* p = MEMROWS ? mb + (size_t)r * 3 * HD : xb + (size_t)r * 3 * HD; qs[r][tid] = bf2f(p[h * DH + tid]); }
    __syncthreads();
    const float scale = 0.08838834764831845f;
    for (int i = tid; i < NR * 40; i += 128) { int r = i / 40, c = i % 40; float s = 0; for (int e = 0; e < DH; ++e) s += qs[r][e] * ks[c][e];
        s = s * scale; bool ok = MEMROWS || (r + 8 >= c); ps[r][c] = ok ? s : -1e9f; }
    __syncthreads();
    if (tid < NR) { float m = -1e30f; for (int c = 0; c < 40; ++c) m = fmaxf(m, ps[tid][c]); float l = 0; for (int c = 0; c < 40; ++c) { float e = __expf(ps[tid][c] - m); ps[tid][c] = e; l += e; }
        for (int c = 0; c < 40; ++c) ps[tid][c] = rbf(ps[tid][c] / l); }
    __syncthreads();
    for (int r = 0; r < NR; ++r) { float s = 0; for (int c = 0; c < 40; ++c) s += ps[r][c] * vs[c][tid];
        if (MEMROWS) out[(size_t)r * HD + h * DH + tid] = f2bf(s); else out[((size_t)seg * 32 + r) * HD + h * DH + tid] = f2bf(s); }
}

extern "C" void kernel_launch(void* const* d_in, const int* in_sizes, int n_in, void* d_out, int out_size, void* d_ws, size_t ws_size, hipStream_t stream) {
    const u16* x = (const u16*)d_in[0]; const u16* mem0 = (const u16*)d_in[1];
    const u16* Wq = (const u16*)d_in[2]; const u16* Wk = (const u16*)d_in[3]; const u16* Wv = (const u16*)d_in[4]; const u16* Wo = (const u16*)d_in[5];
    const u16* w1 = (const u16*)d_in[8]; const u16* w2 = (const u16*)d_in[9];
    u16* out = (u16*)d_out;
    char* ws = (char*)d_ws;
    u16* xn = (u16*)ws; ws += (size_t)S * D * 2;
    u16* qkvx = (u16*)ws; ws += (size_t)S * 3 * HD * 2;
    u16* qkvm = (u16*)ws; ws += (size_t)NSEG * 8 * 3 * HD * 2;
    u16* om = (u16*)ws; ws += (size_t)8 * HD * 2;
    u16* mem = (u16*)ws; ws += (size_t)8 * D * 2;
    u16* ox = (u16*)ws; ws += (size_t)S * HD * 2;
    u16* x1 = (u16*)ws; ws += (size_t)S * D * 2;
    u16* x2 = (u16*)ws; ws += (size_t)S * D * 2;
    u16* hb = (u16*)ws; ws += (size_t)S * F * 2;
    const u16* W[3] = {Wq, Wk, Wv};
    const u16* xin = x;
    for (int layer = 0; layer < 2; ++layer) {
        ln_kernel<<<S, 64, 0, stream>>>(xin, xn);
        for (int j = 0; j < 3; ++j) gemm_kernel<0><<<dim3(HD / 64, S / 64), 256, 0, stream>>>(xn, D, W[j], HD, qkvx + j * HD, 3 * HD, nullptr, S, HD, D);
        for (int s = 0; s < NSEG; ++s) {
            const u16* mcur = s == 0 ? mem0 : mem;
            for (int j = 0; j < 3; ++j) gemm_kernel<0><<<dim3(HD / 64, 1), 256, 0, stream>>>(mcur, D, W[j], HD, qkvm + (size_t)s * 8 * 3 * HD + j * HD, 3 * HD, nullptr, 8, HD, D);
            if (s + 1 < NSEG) {
                attn_kernel<true><<<dim3(H, 1), 128, 0, stream>>>(qkvx, qkvm, om, s);
                gemm_kernel<0><<<dim3(D / 64, 1), 256, 0, stream>>>(om, HD, Wo, D, mem, D, nullptr, 8, D, HD);
            }
        }
        attn_kernel<false><<<dim3(H, NSEG), 128, 0, stream>>>(qkvx, qkvm, ox, 0);
        gemm_kernel<2><<<dim3(D / 64, S / 64), 256, 0, stream>>>(ox, HD, Wo, D, x1, D, xin, S, D, HD);
        ln_kernel<<<S, 64, 0, stream>>>(x1, xn);
        gemm_kernel<1><<<dim3(F / 64, S / 64), 256, 0, stream>>>(xn, D, w1, F, hb, F, nullptr, S, F, D);
        u16* xo = layer == 0 ? x2 : out;
        gemm_kernel<2><<<dim3(D / 64, S / 64), 256, 0, stream>>>(hb, F, w2, D, xo, D, x1, S, D, F);
        xin = xo;
    }
}
```

```cpp
#include <hip/hip_runtime.h>
#include <stdint.h>

#define DELTA 9
#define RUNAHEAD 16
#define WARM 1
#define NAP_Q 2
#define NAP_O 4
#define NAP_M1 6
#define NAP_M2 7
#define NAP_L1 6
#define GAS __attribute__((address_space(1)))
#define LAS __attribute__((address_space(3)))
#define DEV __device__ __forceinline__
typedef unsigned u32;
typedef unsigned short u16;
typedef u32 v2u __attribute__((ext_vector_type(2)));
typedef u32 v4u __attribute__((ext_vector_type(4)));
typedef u32 v8u __attribute__((ext_vector_type(8)));
typedef float f32x2 __attribute__((ext_vector_type(2)));
typedef float f32x4 __attribute__((ext_vector_type(4)));
typedef short v4s __attribute__((ext_vector_type(4)));
typedef __bf16 bf16x2 __attribute__((ext_vector_type(2)));
typedef __bf16 bf16x8 __attribute__((ext_vector_type(8)));
typedef __bf16 bf16x16 __attribute__((ext_vector_type(16)));
typedef __amdgpu_buffer_rsrc_t rsrc_t;

#define NAP_B 0
#define PROBES 2
#define ABL_A 0
#define ABL_B 0
#define ABL_C 0
#define TLBW 0
#define PLACE 0
#define COPIES 1
#define SPEC_AT 1
#define A_TICKS 108
#define SKIP 64
#define POLL_SLEEP ""

constexpr int SEQ = 2048, DM = 640, NH = 32, HD = 4096, NSEG = 64, FF = 1728;
constexpr int SC1 = 16;
constexpr int LDS_BYTES = 163840;
constexpr u32 SPIN_CAP = 400000;

constexpr int FL_STRIDE = 128;
constexpr int F_QK = 0, F_V = 128, F_ATT = 150  , F_L = 256;
static_assert(F_L * FL_STRIDE == 4096 * 8, "role A compares the two chain offsets");
constexpr int F_O = 512, F_R1 = 552, F_M2 = 640, F_M1 = 672  , F_R2 = 512  ;
constexpr int F_R0 = 768, F_DUMP = 848  , F_ABORT = 960, F_PS = 961;
constexpr int RING = 4;
constexpr int OFF_HINT = 131072;
constexpr int OFF_MEM = OFF_HINT + 16384;
constexpr int MEM_SLOT = 98304;
constexpr int OFF_OM = OFF_MEM + 2 * RING * MEM_SLOT;
constexpr int OM_SLOT = 131072;
constexpr int CLEAR_BYTES = OFF_OM + 2 * RING * OM_SLOT;
static_assert(CLEAR_BYTES <= (2 << 20), "one window");
__host__ __device__ constexpr int mem_kb(int r, int ks) { return ((3 * r + (ks >> 2)) >> 1) * 16384 + ((3 * r + (ks >> 2)) & 1) * 4096 + (ks & 3) * 1024; }
constexpr int OFF_KVM = 2 << 20, KVM_RING = 4, KVM_SLOT = 16 * 16384;
constexpr int OFF_XN = 4 << 20;
constexpr int XSZ = SEQ * DM * 2;
constexpr int OFF_X1 = OFF_XN + 2 * XSZ;
constexpr int OFF_XN1 = OFF_X1 + 2 * XSZ;
constexpr int OFF_X2 = OFF_XN1 + 2 * XSZ;
constexpr int OFF_QKV = OFF_X2 + XSZ;
constexpr int QKV_SEG = NH * 24576;
#define SEP_QKV 0
#define SEP_REST 0
constexpr int QKV_L = SEP_QKV * NSEG * QKV_SEG;
constexpr int OFF_OB = OFF_QKV + QKV_L + (NSEG + 1) * QKV_SEG;
constexpr int OB_SEG = 262144;
constexpr int OB_L = SEP_REST * NSEG * OB_SEG;
constexpr int OFF_PO = OFF_OB + OB_L + NSEG * OB_SEG;
constexpr int PART = 32 * DM * 4;
constexpr int PO_L = SEP_REST * NSEG * 4 * PART;
constexpr int OFF_HB = OFF_PO + PO_L + NSEG * 4 * PART;
constexpr int HB_SEG = 54 * 2048;
constexpr int HB_L = SEP_REST * NSEG * HB_SEG;
constexpr int OFF_PM = OFF_HB + HB_L + NSEG * HB_SEG;
constexpr int PM_L = SEP_REST * NSEG * 2 * PART;
constexpr int OFF_DBG = OFF_PM + PM_L + NSEG * 2 * PART;
constexpr int WS_USED = OFF_DBG + (8 << 20);
static_assert(CLEAR_BYTES <= OFF_XN, "clear region");
static_assert(WS_USED + (2 << 20) < (256 << 20), "workspace");
constexpr int OOB = 0x3ff00000;

struct Params {
    const u16* x; const u16* mem0; const u16* Wq; const u16* Wk; const u16* Wv; const u16* Wo; const u16* w1; const u16* w2;
    u16* out; unsigned char* ws;
};

DEV f32x4 mfma32(v4u a, v4u b, f32x4 c) { return __builtin_amdgcn_mfma_f32_16x16x32_bf16(__builtin_bit_cast(bf16x8, a), __builtin_bit_cast(bf16x8, b), c, 0, 0, 0); }
DEV f32x4 smf(v4u a, v8u b, f32x4 c, int idx) { return __builtin_amdgcn_smfmac_f32_16x16x64_bf16(__builtin_bit_cast(bf16x8, a), __builtin_bit_cast(bf16x16, b), c, idx, 0, 0); }
#define MF32 "v_mfma_f32_16x16x32_bf16 "
#define SMF "v_smfmac_f32_16x16x64_bf16 "
DEV void mfa_wb(f32x4& c, const v4u& w, const v4u& b) { asm volatile(MF32 "%0, %1, %2, %0" : "+v"(c) : "a"(w), "v"(b)); }
DEV void mfa_wb0(f32x4& c, const v4u& w, const v4u& b) { asm volatile(MF32 "%0, %1, %2, 0" : "=&v"(c) : "a"(w), "v"(b)); }
DEV void mfa_bw(f32x4& c, const v4u& w, const v4u& b) { asm volatile(MF32 "%0, %2, %1, %0" : "+v"(c) : "a"(w), "v"(b)); }
DEV void mfa_bw0(f32x4& c, const v4u& w, const v4u& b) { asm volatile(MF32 "%0, %2, %1, 0" : "=&v"(c) : "a"(w), "v"(b)); }
DEV void mfv(f32x4& c, const v4u& a, const v4u& b) { asm volatile(MF32 "%0, %1, %2, %0" : "+v"(c) : "v"(a), "v"(b)); }
DEV void mfv0(f32x4& c, const v4u& a, const v4u& b) { asm volatile(MF32 "%0, %1, %2, 0" : "=&v"(c) : "v"(a), "v"(b)); }
DEV void smfa(f32x4& c, const v4u& a, const v8u& w, int idx) { asm volatile(SMF "%0, %1, %2, %3" : "+v"(c) : "v"(a), "a"(w), "v"(idx)); }
DEV void smfv(f32x4& c, const v4u& a, const v8u& w, int idx) { asm volatile(SMF "%0, %1, %2, %3" : "+v"(c) : "v"(a), "v"(w), "v"(idx)); }
#define PIN() __builtin_amdgcn_sched_barrier(0)
#define PAD(n) do { PIN(); asm volatile("s_nop " #n); PIN(); } while (0)
DEV void settle2(f32x4& a, f32x4& b) { PAD(7); asm volatile("" : "+v"(a), "+v"(b)); }
DEV void settle3(f32x4& a, f32x4& b, f32x4& c) { PAD(7); asm volatile("" : "+v"(a), "+v"(b), "+v"(c)); }
DEV void settle4(f32x4& a, f32x4& b, f32x4& c, f32x4& d) { PAD(7); asm volatile("" : "+v"(a), "+v"(b), "+v"(c), "+v"(d)); }
DEV void settle6(f32x4& a, f32x4& b, f32x4& c, f32x4& d, f32x4& e, f32x4& f) { PAD(7); asm volatile("" : "+v"(a), "+v"(b), "+v"(c), "+v"(d), "+v"(e), "+v"(f)); }
template <class T> DEV void opaque(T& x) { asm volatile("" : "+v"(x)); }
template <class T> DEV void opaque_s(T& x) { asm volatile("" : "+s"(x)); }
template <int LEAD> DEV void next_item(int& c, int& s) {
    int a, b;
    asm volatile("s_sub_i32 %2, %4, %1\n\ts_sub_i32 %3, %1, %5\n\ts_cmp_lg_u32 %0, 0\n\ts_cselect_b32 %2, %2, %3\n\ts_cselect_b32 %3, %6, %7\n\t"
                 "s_cmp_gt_i32 %2, 0\n\ts_cselect_b32 %3, %3, 1\n\ts_cselect_b32 %2, 1, 0\n\ts_add_i32 %1, %1, %3\n\ts_xor_b32 %0, %0, %2"
                 : "+s"(c), "+s"(s), "=&s"(a), "=&s"(b) : "n"(NSEG - LEAD), "n"(LEAD - 2), "n"(LEAD), "n"(1 - LEAD) : "scc");
}
DEV int missing(int f, int target, int lim, int other) {
    int r;
    asm volatile("v_cmp_gt_i32 vcc, %2, %1\n\ts_cmp_lg_u64 vcc, 0\n\ts_cselect_b32 %0, %5, 0\n\ts_cmp_gt_i32 %3, %2\n\ts_cselect_b32 %0, %0, %4"
                 : "=&s"(r) : "v"(f), "s"(target), "s"(lim), "s"(other), "s"(OOB) : "vcc", "scc");
    return r;
}
#define SOP(name, ins, CB) DEV int name(int a, int b) { int r; asm volatile(ins " %0, %1, %2" : "=s"(r) : "s"(a), CB(b) : "scc"); return r; }
SOP(s_add, "s_add_i32", "s") SOP(s_addi, "s_add_i32", "n") SOP(s_muli, "s_mul_i32", "n") SOP(s_andi, "s_and_b32", "n") SOP(s_shli, "s_lshl_b32", "n")
SOP(s_shri, "s_lshr_b32", "n") SOP(s_shr, "s_lshr_b32", "s") SOP(s_bfei, "s_bfe_u32", "n") SOP(s_minu, "s_min_u32", "n")
DEV float vmax3(float a, float b, float c) { float r; asm("v_max3_f32 %0, %1, %2, %3" : "=v"(r) : "v"(a), "v"(b), "v"(c)); return r; }
DEV float vmax2(float a, float b) { float r; asm("v_max_f32 %0, %1, %2" : "=v"(r) : "v"(a), "v"(b)); return r; }
DEV u32 pk2(float a, float b) { bf16x2 v = {(__bf16)a, (__bf16)b}; return __builtin_bit_cast(u32, v); }
DEV float bflo(u32 v) { return __uint_as_float(v << 16); }
DEV float bfhi(u32 v) { return __uint_as_float(v & 0xffff0000u); }
DEV float rbf(float a) { return bflo(pk2(a, a)); }
DEV v4u ld16(rsrc_t r, int voff, int soff) { return __builtin_amdgcn_raw_buffer_load_b128(r, voff, soff, SC1); }
DEV v2u ld8(rsrc_t r, int voff, int soff) { return __builtin_amdgcn_raw_buffer_load_b64(r, voff, soff, SC1); }
DEV u32 ld4(rsrc_t r, int voff, int soff) { return __builtin_amdgcn_raw_buffer_load_b32(r, voff, soff, SC1); }
DEV void st16(v4u v, rsrc_t r, int voff, int soff) {
    __builtin_amdgcn_raw_buffer_store_b128(v, r, voff, soff, SC1);
    asm volatile("s_nop 1" ::"v"(v));
}
DEV void st4(u32 v, rsrc_t r, int voff, int soff) { __builtin_amdgcn_raw_buffer_store_b32(v, r, voff, soff, SC1); }
DEV v4u lds16(u32 a) { return *(const LAS v4u*)a; }
DEV void lds16w(u32 a, v4u v) { *(LAS v4u*)a = v; }
DEV v2u ldtr(u32 a) { return __builtin_bit_cast(v2u, __builtin_amdgcn_ds_read_tr16_b64_v4i16((LAS v4s*)a)); }
DEV void drain() { asm volatile("s_waitcnt vmcnt(0)" ::: "memory"); }
DEV void bar() { asm volatile("s_waitcnt lgkmcnt(0)\n\ts_barrier" ::: "memory"); }
DEV void cfence() { asm volatile("" ::: "memory"); }
DEV u32 umax3(u32 a, u32 b, u32 c) { return max(max(a, b), c); }
DEV u32 vmax4(u32 m, v4u d) { return umax3(umax3(m, d.x, d.y), d.z, d.w); }
DEV void glds16(const void* gsrc, u32 lds_dst) {
    u32 keep;
    asm volatile("s_mov_b32 %0, m0\n\ts_mov_b32 m0, %2\n\ts_nop 0\n\tglobal_load_lds_dwordx4 %1, off\n\ts_mov_b32 m0, %0" : "=&s"(keep) : "v"(gsrc), "s"(lds_dst) : "memory");
}
DEV void dma_base(u32 lds_dst) { asm volatile("s_mov_b32 m0, %0\n\ts_nop 0" ::"s"(lds_dst) : "memory"); }
DEV void dma_base0(u32 lds_dst) { asm volatile("s_mov_b32 m0, %0" ::"s"(lds_dst) : "memory"); }
template <int V> struct IC { static constexpr int value = V; };
template <int OFF> DEV void dma16(rsrc_t r, int voff, int soff) {
    asm volatile("buffer_load_dwordx4 %0, %1, %2 offen offset:%3 sc1 lds" ::"v"(voff), "s"(r), "s"(soff), "n"(OFF) : "memory");
}
template <int N> DEV void waitvm() { asm volatile("s_waitcnt vmcnt(%0)" ::"n"(N) : "memory"); }
DEV void waitvm_n(int n) {
    switch (n) {
#define WCASE(k) case k: waitvm<k>(); break;
        WCASE(1) WCASE(2) WCASE(3) WCASE(4) WCASE(5) WCASE(6) WCASE(7) WCASE(8) WCASE(9) WCASE(10) WCASE(11) WCASE(12) WCASE(13) WCASE(14) WCASE(15) WCASE(16)
        WCASE(17) WCASE(18) WCASE(19) WCASE(20) WCASE(21) WCASE(22) WCASE(23) WCASE(24)
        default: waitvm<0>();
    }
}
template <int CTRL> DEV u32 dppu(u32 x) { return (u32)__builtin_amdgcn_mov_dpp((int)x, CTRL, 0xf, 0xf, true); }
template <int CTRL> DEV float dppf(float x) { return __uint_as_float(dppu<CTRL>(__float_as_uint(x))); }
DEV float xrow_max(float x) {
    float y;
    asm("v_mov_b32 %1, %0\n\ts_nop 1\n\tv_permlane16_swap_b32 %0, %1\n\tv_max_f32 %0, %0, %1\n\tv_mov_b32 %1, %0\n\ts_nop 1\n\tv_permlane32_swap_b32 %0, %1\n\tv_max_f32 %0, %0, %1"
        : "+v"(x), "=&v"(y));
    return x;
}
DEV float xrow_sum(float x) {
    auto s = __builtin_amdgcn_permlane16_swap(__float_as_uint(x), __float_as_uint(x), false, false);
    x = __uint_as_float(s[0]) + __uint_as_float(s[1]);
    auto t = __builtin_amdgcn_permlane32_swap(__float_as_uint(x), __float_as_uint(x), false, false);
    return __uint_as_float(t[0]) + __uint_as_float(t[1]);
}
DEV float wave_sum(float x) {
    x += dppf<0xB1>(x); x += dppf<0x4E>(x); x += dppf<0x141>(x); x += dppf<0x128>(x);
    return xrow_sum(x);
}
DEV void wave_sum2(float& a, float& b) {
    a += dppf<0xB1>(a); b += dppf<0xB1>(b); a += dppf<0x4E>(a); b += dppf<0x4E>(b); a += dppf<0x141>(a); b += dppf<0x141>(b); a += dppf<0x128>(a); b += dppf<0x128>(b);
    auto s = __builtin_amdgcn_permlane16_swap(__float_as_uint(a), __float_as_uint(b), false, false);
    const float x = __uint_as_float(s[0]) + __uint_as_float(s[1]);
    auto t = __builtin_amdgcn_permlane32_swap(__float_as_uint(x), __float_as_uint(x), false, false);
    const u32 y = __float_as_uint(__uint_as_float(t[0]) + __uint_as_float(t[1]));
    a = __uint_as_float(__builtin_amdgcn_readlane(y, 0)); b = __uint_as_float(__builtin_amdgcn_readlane(y, 16));
}
#define GIVE_UP(cnt, t, ws, ab, code) \
    "s_sub_u32 " cnt ", " cnt ", 1\n\ts_cbranch_scc1 3f\n\ts_and_b32 vcc_lo, " cnt ", 127\n\ts_cbranch_scc1 1b\n\t" \
    "buffer_load_dword " t ", off, " ws ", " ab " sc1\n\ts_waitcnt vmcnt(0)\n\tv_cmp_ne_u32 vcc, -1, " t "\n\ts_cbranch_vccz 1b\n\ts_endpgm\n" \
    "3:\n\tv_mov_b32 " t ", " code "\n\tbuffer_store_dword " t ", off, " ws ", " ab " sc1\n\ts_endpgm\n2:"
template <int CODE> DEV void wait_flags(rsrc_t ws, int voff, int target) {
    u32 cnt = SPIN_CAP; int f;
    asm volatile("1:\n\tbuffer_load_dword %1, %2, %3, 0 offen sc1\n\ts_waitcnt vmcnt(0)\n\tv_cmp_gt_i32 vcc, %4, %1\n\ts_cbranch_vccz 2f\n\t" POLL_SLEEP
                 GIVE_UP("%0", "%1", "%3", "%5", "%6")
                 : "+s"(cnt), "=&v"(f) : "v"(voff), "s"(ws), "s"(target), "s"(F_ABORT * FL_STRIDE), "n"(CODE) : "vcc", "scc", "memory");
}
template <int CODE> DEV void spin_check(u32& cnt, rsrc_t ws) {
    int t;
    asm volatile("s_branch 4f\n1:\n\ts_branch 2f\n4:\n\t" GIVE_UP("%0", "%1", "%2", "%3", "%4")
                 : "+s"(cnt), "=&v"(t) : "s"(ws), "s"(F_ABORT * FL_STRIDE), "n"(CODE) : "vcc", "scc", "memory");
}
DEV void set_word(rsrc_t ws, int off, int val, int lane) { st4((u32)val, ws, lane == 0 ? 0 : OOB, off); }

#define STAMP_DECL do { } while (0)
#define STAMP2(it, k) do { } while (0)
#define STAMPA(it, k) do { } while (0)
#define STAMP_DUMP() do { } while (0)

#define SACC_DECL do { } while (0)
#define SACC_AT(k) do { } while (0)
#define SACC_DUMP() do { } while (0)
#define CLK_EXIT() do { } while (0)
#define MARK(k) do { } while (0)
DEV v4u pack_swap(u32 a0, u32 a1, u32 b0, u32 b1) {
    auto s0 = __builtin_amdgcn_permlane16_swap(a0, b0, false, false);
    auto s1 = __builtin_amdgcn_permlane16_swap(a1, b1, false, false);
    return (v4u){s0[0], s1[0], s0[1], s1[1]};
}

template <int KS, int NT, int CH, class Src>
DEV void load_dense_w(v4u (&W)[NT][KS], Src src, int w, int lane) {
    constexpr int UPR = NT * 8;
    constexpr int RB = UPR * 16;
    constexpr int NCH = (KS + CH - 1) / CH;
    constexpr int BUF = CH * 32 * RB;
    static_assert(2 * BUF <= LDS_BYTES, "weight staging");
    auto swz = [](int row) { return NT == 2 ? ((row & 3) | ((row >> 1) & 4)) << 1 : (((row >> 1) & 1) | ((row >> 2) & 2)) << 1; };
    auto issue = [&](int c) {
        const int rows = (c == NCH - 1 ? KS - c * CH : CH) * 32;
        const int ninst = rows * UPR / 256;
        for (int i = 0; i < ninst; ++i) {
            const int u = (i * 4 + w) * 64 + lane;
            const int row = u / UPR;
            glds16(src(c * CH * 32 + row, (u % UPR) ^ swz(row)), (u32)((c & 1) * BUF + (i * 4 + w) * 1024));
        }
    };
    issue(0);
    const int g = lane >> 4, q = (lane >> 2) & 3, pp = lane & 3;
    u32 rd[NT];
#pragma unroll
    for (int t = 0; t < NT; ++t) rd[t] = (u32)((8 * g + q) * RB + (((w * NT + t) * 2) ^ swz(8 * g + q)) * 16 + pp * 8);
#pragma unroll
    for (int c = 0; c < NCH; ++c) {
        if (c + 1 < NCH) {
            issue(c + 1);
            constexpr int dummy = 0; (void)dummy;
            if (c + 1 == NCH - 1) waitvm<(KS - (NCH - 1) * CH) * 32 * UPR / 256>(); else waitvm<CH * 32 * UPR / 256>();
        } else drain();
        bar();
#pragma unroll
        for (int k = 0; k < CH; ++k) {
            if (c * CH + k < KS) {
#pragma unroll
                for (int t = 0; t < NT; ++t) {
                    const u32 a = rd[t] + (c & 1) * BUF + k * 32 * RB;
                    const v2u lo = ldtr(a), hi = ldtr(a + 4 * RB);
                    W[t][c * CH + k] = (v4u){lo.x, lo.y, hi.x, hi.y};
                }
            }
        }
        bar();
    }
}

template <int KS, int NT, class Hook>
DEV void gemm_tile(f32x4 (&acc)[NT][2], const v4u (&W)[NT][KS], u32 buf, int pos, Hook hook) {
    u32 re = buf + (u32)(pos * 16);
    opaque(re);
    v4u b[3][2];
#pragma unroll
    for (int k = 0; k < 2; ++k) { b[k][0] = lds16(re + k * 2048); b[k][1] = lds16(re + k * 2048 + 1024); }
    PIN();
#pragma unroll
    for (int ks = 0; ks < KS; ++ks) {
        hook(ks);
        PIN();
#pragma unroll
        for (int m = 0; m < 2; ++m) {
#pragma unroll
            for (int i = 0; i < NT; ++i) {
                if (ks == 0) mfa_wb0(acc[i][m], W[i][ks], b[ks % 3][m]); else mfa_wb(acc[i][m], W[i][ks], b[ks % 3][m]);
                if (i == 0 && ks + 2 < KS) { b[(ks + 2) % 3][m] = lds16(re + (ks + 2) * 2048 + m * 1024); PIN(); }
            }
        }
        PIN();
    }
    if constexpr (NT == 3) settle6(acc[0][0], acc[0][1], acc[1][0], acc[1][1], acc[2][0], acc[2][1]);
    else settle4(acc[0][0], acc[0][1], acc[1][0], acc[1][1]);
}

DEV int page_sel(rsrc_t ws, int& ps2) {
    wait_flags<0x700>(ws, F_PS * FL_STRIDE, 0);
    const int v = __builtin_amdgcn_readfirstlane(ld4(ws, F_PS * FL_STRIDE, 0));
    ps2 = (v & 1) * 8192;
    return v & 8192;
}
DEV int quad_pos(int g, int t) { return (t >> 2) * 16 + ((2 * (g & 1) + (g >> 1)) ^ (t >> 3)) * 4 + (t & 3); }
template <bool VMODE>
DEV void role_qkv(const Params& p, rsrc_t ws, int nsl, int w, int lane) {
    STAMP_DECL;
    const int tile0 = nsl * 12;
    if (!VMODE && nsl < 21) for (int i = 0; i < NAP_Q; ++i) __builtin_amdgcn_s_sleep(32);
    v4u W[3][20];
    load_dense_w<20, 3, 5>(W, [&](int row, int unit) -> const void* {
        int col = tile0 * 16 + unit * 8;
        if (VMODE) { col = min(col, HD - 8); return p.Wv + (size_t)row * HD + col; }
        col = min(col, 2 * HD - 8);
        return (col < HD ? p.Wq : p.Wk - HD) + (size_t)row * HD + col;
    }, w, lane);
    MARK(0);
    const int g = lane >> 4, t = lane & 15;
    int so[3];
#pragma unroll
    for (int i = 0; i < 3; ++i) {
        const int T = tile0 + w * 3 + i, Th = T & 255, tau = Th & 7;
        if (VMODE) so[i] = T < 256 ? (T >> 3) * 24576 + 16384 + (T & 7) * 1024 + lane * 16 : OOB;
        else so[i] = T < 512 ? (Th >> 3) * 24576 + (T >> 8) * 8192 + ((tau >> 1) * 2 + (g & 1)) * 1024 + ((2 * (tau & 1) + (g >> 1)) * 16 + t) * 16 : OOB;
    }
    int ps2;
    const int ps = page_sel(ws, ps2);
    const int fme = ((VMODE ? F_V : F_QK) + nsl) * FL_STRIDE + ps, fdump = (F_DUMP + nsl + (VMODE ? 43 : 0)) * FL_STRIDE;
    int t_src, t_line, t_want, t_dst, t_flag, t_line2, t_want2;
    auto tables = [&](int hf) {
        auto entry = [&](int n) {
            const int m = n - RUNAHEAD, grp = m / (2 * SKIP - 1), r = m % (2 * SKIP - 1), n0 = RUNAHEAD + grp * (SKIP - 1) + r / 2;
            return m < 0 ? n : n0 >= NSEG ? 128 + n - NSEG : (r & 1) ? n0 : 128 + grp * SKIP + r / 2;
        };
        auto line = [&](int e) { return e >= 128 ? F_R2 * FL_STRIDE + (ps ^ 8192) : (F_R0 + ((e & 1) | (e >= 16)) * 8) * FL_STRIDE; };
        const int i = hf * 64 + lane;
        const int e = entry(i + 1), e2 = entry(i + 2), l = e >> 7, sg = e & 127;
        t_src = OFF_XN + l * XSZ + sg * 40960 + w * 4096;
        t_line = line(e); t_want = sg; t_line2 = line(e2); t_want2 = e2 & 127;
        t_dst = OFF_QKV + l * QKV_L + sg * QKV_SEG;
        t_flag = fme + l * F_L * FL_STRIDE;
    };
    tables(0);
    const int fl8 = (lane & 7) * FL_STRIDE, l16 = lane * 16, one = (w == 0 && lane == 0) ? 0 : OOB;
    const int back = 4096 * w - 2048 * w;
    auto pack = [&](const f32x4 (&a)[2]) {
        const u32 a0 = pk2(a[0][0], a[0][1]), a1 = pk2(a[0][2], a[0][3]), b0 = pk2(a[1][0], a[1][1]), b1 = pk2(a[1][2], a[1][3]);
        return VMODE ? (v4u){a0, a1, b0, b1} : pack_swap(a0, a1, b0, b1);
    };
    auto fetch_all = [&](int src, u32 dst) {
        dma_base(dst + w * 4096); dma16<0>(ws, l16, src); dma16<1024>(ws, l16, src); dma16<2048>(ws, l16, src); dma16<3072>(ws, l16, src);
        dma_base(dst + w * 4096 + 16384); src += 16384; dma16<0>(ws, l16, src); dma16<1024>(ws, l16, src); dma16<2048>(ws, l16, src); dma16<3072>(ws, l16, src);
        dma_base(dst + w * 2048 + 32768); src += 16384 - back; dma16<0>(ws, l16, src); dma16<1024>(ws, l16, src);
    };
    constexpr u32 CNT = 2 * 40960;
    u32 rd = (u32)(quad_pos(g, t) * 16), mw = CNT + w * 4;
    u32 rdb[2] = {rd, rd + 40960};
    opaque(rdb[0]); opaque(rdb[1]); opaque(mw);
    int m0v[2][3];
#pragma unroll
    for (int k = 0; k < 6; ++k) { m0v[k / 3][k % 3] = (k / 3) * 40960 + (k % 3) * 16384 + w * (k % 3 == 2 ? 2048 : 4096); opaque_s(m0v[k / 3][k % 3]); }
    v4u b[4][2];
    wait_flags<0x103>(ws, F_R0 * FL_STRIDE + fl8, 0);
    fetch_all(OFF_XN + w * 4096, 0);
    drain(); bar();
#pragma unroll
    for (int k = 0; k < 4; ++k) b[k >> 1][k & 1] = lds16(rdb[0] + k * 1024);
    u32 cyc0 = 0; (void)cyc0;
    constexpr int DUMP = OFF_QKV + QKV_L + NSEG * QKV_SEG;
    int n = 0, seg = 0, csrc = OFF_XN + w * 4096, cdst = OFF_QKV, cflag = fme, pdst = DUMP, pflag = fdump, pseg = 0, f2 = 0;
    int miss = 1;
    auto task = [&](auto Bc, f32x4 (&acc)[3][2], f32x4 (&prev)[3][2]) {
        constexpr int B = decltype(Bc)::value;
        STAMP2(n, 0);
        int line, want, line2, want2, nsrc, ndst, nflag, f, nmiss, src; v4u o, all;
        auto gap = [&](int i) {
            switch (i) {
                case 0: nsrc = __builtin_amdgcn_readlane(t_src, n); opaque_s(nsrc); dma_base0(m0v[B ^ 1][0]); break;
                case 1:
                    asm volatile("s_cmp_lg_u32 %1, 0\n\ts_cselect_b32 %0, %2, %3" : "=s"(src) : "s"(miss), "s"(csrc), "s"(nsrc) : "scc"); break;
                case 2: case 10: case 18: dma16<0>(ws, l16, src); break;
                case 4: case 12: case 20: dma16<1024>(ws, l16, src); break;
                case 6: case 14: dma16<2048>(ws, l16, src); break;
                case 8: case 16: dma16<3072>(ws, l16, src); break;
                case 9: src = s_addi(src, 16384); dma_base0(m0v[B ^ 1][1]); break;
                case 17: src = s_add(src, 16384 - back); dma_base0(m0v[B ^ 1][2]); break;
                case 21: *(LAS int*)(mw + B * 16) = miss; break;
                case 22: case 26: case 30: asm volatile("" : "+v"(prev[i / 4 - 5][0]), "+v"(prev[i / 4 - 5][1])); o = pack(prev[i / 4 - 5]); opaque(o); break;
                case 24: case 28: case 32: __builtin_amdgcn_raw_buffer_store_b128(o, ws, so[i / 4 - 6], pdst, SC1); break;
                case 25: case 29: case 33: asm volatile("" ::"v"(o)); break;
                case 34: line2 = __builtin_amdgcn_readlane(t_line2, n); opaque_s(line2); break;
                case 35: want2 = __builtin_amdgcn_readlane(t_want2, n); opaque_s(want2); break;
                case 36: line = __builtin_amdgcn_readlane(t_line, n); opaque_s(line); break;
                case 37: want = __builtin_amdgcn_readlane(t_want, n); opaque_s(want); break;
                case 38: ndst = __builtin_amdgcn_readlane(t_dst, n); opaque_s(ndst); break;
                case 39: nflag = __builtin_amdgcn_readlane(t_flag, n); opaque_s(nflag); break;
                case 44: asm volatile("buffer_load_dword v201, %1, %2, %3 offen sc1" : "={v201}"(f) : "v"(fl8), "s"(ws), "s"(line2) : "memory"); break;
                case 60: asm volatile("buffer_load_dword v200, %1, %2, %3 offen sc1" : "+{v200}"(f2) : "v"(fl8), "s"(ws), "s"(line) : "memory"); break;
                case 71:
                    waitvm<1>(); bar(); all = lds16(CNT + B * 16); break;
                case 72: asm volatile("v_cmp_gt_i32 vcc, %1, %2\n\ts_cmp_lg_u64 vcc, 0\n\ts_cselect_b32 %0, 1, 0" : "=&s"(nmiss) : "s"(want2), "{v201}"(f) : "vcc", "scc", "memory"); break;
                case 74: st4((u32)pseg, ws, one, pflag); break;
                default: break;
            }
        };
#pragma unroll
        for (int ks = 0; ks < 20; ++ks) {
#pragma unroll
            for (int m = 0; m < 2; ++m)
#pragma unroll
                for (int i = 0; i < 3; ++i) {
                    if (VMODE) { if (ks == 0) mfa_bw0(acc[i][m], W[i][ks], b[ks & 3][m]); else mfa_bw(acc[i][m], W[i][ks], b[ks & 3][m]); }
                    else       { if (ks == 0) mfa_wb0(acc[i][m], W[i][ks], b[ks & 3][m]); else mfa_wb(acc[i][m], W[i][ks], b[ks & 3][m]); }
                    if (i == 0) b[(ks + 2) & 3][m] = lds16((ks < 18 ? rdb[B] + (ks + 2) * 2048 : rdb[B ^ 1] + (ks - 18) * 2048) + m * 1024);
                    else gap(ks * 4 + m * 2 + i - 1);
                    PIN();
                }
        }
        STAMP2(n, 1);
        pdst = cdst; pflag = cflag; pseg = seg;
        if (__builtin_amdgcn_readfirstlane(all.x | all.y | all.z | all.w)) {
            settle6(acc[0][0], acc[0][1], acc[1][0], acc[1][1], acc[2][0], acc[2][1]);
            asm volatile("s_waitcnt vmcnt(1)" : "+{v200}"(f2)::"memory");
            const int still = missing(f2, want, NSEG, OOB);
#pragma unroll
            for (int i = 0; i < 3; ++i) st16(pack(acc[i]), ws, so[i], pdst);
            if (miss && !still) { fetch_all(nsrc, (B ^ 1) * 40960); waitvm<10>(); } else drain();
            bar();
            st4((u32)pseg, ws, one, pflag);
            STAMP2(n, 2);
            if (want >= NSEG) { STAMP_DUMP(); CLK_EXIT(); asm volatile("s_endpgm"); }
            if (miss && still) { wait_flags<0x10b>(ws, fl8 + line, want); STAMP2(n, 3); fetch_all(nsrc, (B ^ 1) * 40960); drain(); } else waitvm<1>();
            bar();
#pragma unroll
            for (int k = 0; k < 4; ++k) b[k >> 1][k & 1] = lds16(rdb[B ^ 1] + k * 1024);
            pdst = DUMP; pflag = fdump;
        }
        seg = want; csrc = nsrc; cdst = ndst; cflag = nflag; miss = nmiss; n = s_addi(n, 1);
    };
    f32x4 accA[3][2] = {}, accB[3][2] = {};
    while (n < 2 * NSEG) {
        task(IC<0>{}, accA, accB); task(IC<1>{}, accB, accA);
        if (__builtin_expect(n == 64, 0)) tables(1);
    }
}

template <int KIND>
DEV void role_gemm(const Params& p, rsrc_t ws, int layer, int kq, int nsl, int w, int lane) {
    STAMP_DECL;
    constexpr int KS = KIND == 0 ? 32 : KIND == 2 ? 27 : 20;
    constexpr int NT = (KIND == 0 || KIND == 2) ? 2 : 3;
    constexpr int CH = KIND == 0 ? 8 : KIND == 2 ? 9 : 5;
    constexpr int NLD = (KS * 2 + 3) / 4;
    constexpr int NST = NT == 2 ? 4 : 3;
    constexpr int LPC = 4;
    constexpr u32 BUF = KS * 2048;
    const int tile0 = nsl * 12;
    { const int naps = (KIND == 0 ? NAP_O : KIND == 1 ? NAP_M1 : NAP_M2) + layer * NAP_L1;
      for (int i = 0; i < naps; ++i) __builtin_amdgcn_s_sleep(32); }
    v4u W[NT][KS];
    load_dense_w<KS, NT, CH>(W, [&](int row, int unit) -> const void* {
        if (KIND == 0) return p.Wo + (size_t)(kq * 1024 + row) * DM + nsl * 128 + unit * 8;
        if (KIND == 1) return p.w1 + (size_t)row * FF + nsl * 192 + unit * 8;
        return p.w2 + (size_t)(kq * 864 + row) * DM + nsl * 128 + unit * 8;
    }, w, lane);
    MARK(0);
    const int g = lane >> 4, t = lane & 15;
    int so[NST];
#pragma unroll
    for (int i = 0; i < NST; ++i) {
        const int T = tile0 + w * 3 + i;
        constexpr int NR = KIND == 0 ? 4 : 8;
        if (NT == 2) { const int row = (i & 1) * 16 + t; so[i] = (row / NR) * (NR * 2560) + (nsl * 8 + w * 2 + (i >> 1)) * (NR * 64) + (row % NR) * 64 + g * 16; }
        else so[i] = ((T >> 1) * 2 + (g & 1)) * 1024 + ((2 * (T & 1) + (g >> 1)) * 16 + t) * 16;
    }
    int ps2;
    const int ps = page_sel(ws, ps2);
    const int fme = (KIND == 0 ? F_O + layer * 20 + kq * 5 + nsl : KIND == 1 ? F_M1 + layer * 16 + nsl : F_M2 + layer * 16 + kq * 5 + nsl) * FL_STRIDE + (KIND == 1 ? ps ^ 8192 : ps);
    const int fo = (KIND == 0 ? F_ATT + layer * F_L + kq * 8 + (lane & 7) : KIND == 1 ? F_R1 + layer * 8 + (lane & 7) : F_M1 + layer * 16 + kq * 4 + min(lane, 4)) * FL_STRIDE + (KIND == 1 ? ps : ps ^ 8192);
    const int src0 = KIND == 0 ? OFF_OB + layer * OB_L + kq * 65536 : KIND == 1 ? OFF_XN1 + layer * XSZ : OFF_HB + layer * HB_L + kq * 27 * 2048;
    const int dst0 = KIND == 0 ? OFF_PO + layer * PO_L + kq * PART : KIND == 1 ? OFF_HB + layer * HB_L : OFF_PM + layer * PM_L + kq * PART;
    auto srcof = [&](int s) { return src0 + s * (KIND == 0 ? OB_SEG : KIND == 1 ? 40960 : HB_SEG); };
    auto dstof = [&](int s) { return dst0 + s * (KIND == 0 ? 4 * PART : KIND == 1 ? HB_SEG : 2 * PART); };
    constexpr int GL = NLD % 4 ? NLD % 4 : 4;
    const int wg = w * 4096, wl = min(w * GL, KS * 2 - (NLD - GL) * 4 - GL) * 1024;
    auto fetch = [&](int i, int voff, int& src, u32 dst) {
        if (i % 4 == 0) { const int o = i * 4096 + (i + GL == NLD ? wl : wg); dma_base(dst + o); src += o - (i ? (i - 4) * 4096 + wg : 0); opaque_s(src); }
        if (i % 4 == 0) dma16<0>(ws, voff, src); else if (i % 4 == 1) dma16<1024>(ws, voff, src); else if (i % 4 == 2) dma16<2048>(ws, voff, src); else dma16<3072>(ws, voff, src);
    };
    const int rdpos = NT == 3 ? quad_pos(g, t) : lane;
    wait_flags<0x100 + KIND>(ws, fo, 0);
    constexpr int NUP = 8, NIN = NLD - NUP, LOOK = KS - (NT == 2 ? 8 : 6);
    const int l16 = lane * 16, one = (w == 0 && lane == 0) ? 0 : OOB;
    int csrc = srcof(0);
#pragma unroll
    for (int i = 0; i < NUP; ++i) fetch(i, l16, csrc, 0);
    waitvm<NUP - LPC>(); bar();
    u32 buf = 0;
    for (int seg = 0; seg < NSEG; ++seg) {
        STAMP2(seg, 0);
        int f = 0;
        f32x4 acc[NT][2];
        gemm_tile<KS, NT>(acc, W, buf, rdpos, [&](int ks) {
            if ((ks + 2) % (2 * LPC) == 0 && ks + 2 < KS) {
                const int j = (ks + 2) / (2 * LPC), last = ((j + 1) * LPC < NLD ? (j + 1) * LPC : NLD) - 1, issued = NUP + (ks < NIN ? ks : NIN);
                waitvm_n(issued - 1 - last + (ks > LOOK ? 1 : 0)); bar();
            }
            if (ks < NIN) fetch(NUP + ks, l16, csrc, buf);
            if (ks == LOOK) { cfence(); f = (int)ld4(ws, fo, 0); cfence(); }
        });
        STAMP2(seg, 1);
        int miss = missing(f, seg + 1, NSEG, OOB);
        const int dst = dstof(seg);
        buf ^= BUF; csrc = srcof(seg + 1);
        if (!miss) {
#pragma unroll
            for (int i = 0; i < NUP; ++i) fetch(i, l16, csrc, buf);
        }
        v4u o[NST];
        if (NT == 3) { cfence(); f = (int)ld4(ws, fo, 0); cfence(); }
#pragma unroll
        for (int i = 0; i < NT; ++i) {
            if (NT == 2) { o[2 * i] = __builtin_bit_cast(v4u, acc[i][0]); o[2 * i + 1] = __builtin_bit_cast(v4u, acc[i][1]); }
            else {
                u32 d[2][2];
#pragma unroll
                for (int m = 0; m < 2; ++m)
#pragma unroll
                    for (int r = 0; r < 2; ++r) {
                        const u32 hb = pk2(acc[i][m][2 * r], acc[i][m][2 * r + 1]);
                        const f32x2 h2 = {bflo(hb), bfhi(hb)}, z = h2 * -1.4426950408889634f;
                        const f32x2 e = (f32x2){__builtin_amdgcn_exp2f(z.x), __builtin_amdgcn_exp2f(z.y)} + 1.0f;
                        const f32x2 y = h2 * (f32x2){__builtin_amdgcn_rcpf(e.x), __builtin_amdgcn_rcpf(e.y)};
                        d[m][r] = pk2(y.x, y.y);
                    }
                o[i] = pack_swap(d[0][0], d[0][1], d[1][0], d[1][1]); opaque(o[i]);
            }
        }
        if (NT == 3 && miss) {
            miss = missing(f, seg + 1, NSEG, OOB);
            if (!miss) {
#pragma unroll
                for (int i = 0; i < NUP; ++i) fetch(i, l16, csrc, buf);
            }
        }
#pragma unroll
        for (int i = 0; i < NST; ++i) st16(o[i], ws, so[i], dst);
        drain(); bar();
        st4((u32)seg, ws, one, fme);
        STAMP2(seg, 2);
        if (miss && seg + 1 < NSEG) {
            wait_flags<0x108 + KIND>(ws, fo, seg + 1);
            STAMP2(seg, 3);
#pragma unroll
            for (int i = 0; i < NUP; ++i) fetch(i, l16, csrc, buf);
            waitvm<NUP - LPC>();
        }
        bar();
    }
    STAMP_DUMP();
}

struct Row { f32x2 v[5]; };
DEV u32 pk2(f32x2 a) { return pk2(a.x, a.y); }
DEV f32x2 unpk(u32 d) { return (f32x2){bflo(d), bfhi(d)}; }
template <int AUX> DEV void row_store(const u32 (&y)[5], rsrc_t r, int soff, int lane) {
    const bool odd = lane & 1;
    const u32 n0 = dppu<0xB1>(odd ? y[0] : y[2]), n1 = dppu<0xB1>(odd ? y[1] : y[3]);
    const v4u o = odd ? (v4u){n0, n1, y[2], y[3]} : (v4u){y[0], y[1], n0, n1};
    const v4u o2 = {y[4], dppu<0x55>(y[4]), dppu<0xAA>(y[4]), dppu<0xFF>(y[4])};
    __builtin_amdgcn_raw_buffer_store_b128(o, r, odd ? 512 + (lane - 1) * 8 : lane * 8, soff, AUX);
    __builtin_amdgcn_raw_buffer_store_b128(o2, r, (lane & 3) ? OOB : 1024 + lane * 4, soff, AUX);
    asm volatile("s_nop 1" ::"v"(o), "v"(o2));
}
DEV void row_to_lds(const Row& y, int slot, int lane) {
    const u32 b = (u32)(slot * 1296);
    *(LAS v2u*)(b + lane * 8) = (v2u){pk2(y.v[0]), pk2(y.v[1])};
    *(LAS v2u*)(b + 512 + lane * 8) = (v2u){pk2(y.v[2]), pk2(y.v[3])};
    *(LAS u32*)(b + 1024 + lane * 4) = pk2(y.v[4]);
}
template <int NR> struct FragMap {
    static constexpr int NI = (80 * NR + 255) / 256;
    u32 from[NI]; int to[NI];
    DEV FragMap(int t0, int tid) {
#pragma unroll
        for (int i = 0; i < NI; ++i) {
            const int u = min(i * 256 + tid, 80 * NR - 1), q = (u >> 4) % (NR / 4), ks = (u >> 4) / (NR / 4), j = q * 4 + (u & 3), tk = t0 + j;
            const int gp = ((u >> 2) & 3) ^ ((tk >> 3) & 1), g = (gp >> 1) + 2 * (gp & 1);
            from[i] = (u32)(j * 1296 + (ks * 4 + g) * 16);
            to[i] = i * 256 + tid < 80 * NR ? (ks * 2 + (tk >> 4)) * 1024 + (((tk & 15) >> 2) * 16 + (u & 15)) * 16 : OOB;
        }
    }
    DEV void store(rsrc_t ws, int soff, int slot0) const {
        v4u v[NI];
#pragma unroll
        for (int i = 0; i < NI; ++i) v[i] = lds16(from[i] + slot0 * 1296);
#pragma unroll
        for (int i = 0; i < NI; ++i) st16(v[i], ws, to[i], soff);
    }
};
DEV float row_total(const Row& x) { const f32x2 s = ((x.v[0] + x.v[1]) + (x.v[2] + x.v[3])) + x.v[4]; return s.x + s.y; }
DEV float row_squares(const Row& x, float mean) {
    f32x2 q = {0, 0};
#pragma unroll
    for (int i = 0; i < 5; ++i) { const f32x2 d = x.v[i] - mean; q += d * d; }
    return q.x + q.y;
}
DEV void row_scale(const Row& x, Row& y, float mean, float squares) {
    const float inv = __builtin_amdgcn_rcpf(rbf(__builtin_amdgcn_sqrtf(rbf(rbf(squares * (1.0f / DM)) + 1e-5f)))), mb = rbf(mean);
#pragma unroll
    for (int i = 0; i < 5; ++i) y.v[i] = unpk(pk2(x.v[i] - mb)) * inv;
}
DEV void layer_norm(const Row& x, Row& y) {
    const float mean = wave_sum(row_total(x)) * (1.0f / DM);
    row_scale(x, y, mean, wave_sum(row_squares(x, mean)));
}
DEV void layer_norm2(const Row (&x)[2], Row (&y)[2]) {
    float s0 = row_total(x[0]), s1 = row_total(x[1]);
    wave_sum2(s0, s1);
    s0 *= 1.0f / DM; s1 *= 1.0f / DM;
    float q0 = row_squares(x[0], s0), q1 = row_squares(x[1], s1);
    wave_sum2(q0, q1);
    row_scale(x[0], y[0], s0, q0); row_scale(x[1], y[1], s1, q1);
}
DEV void unpack_row(Row& x, v2u a, v2u b, u32 c) { x.v[0] = unpk(a.x); x.v[1] = unpk(a.y); x.v[2] = unpk(b.x); x.v[3] = unpk(b.y); x.v[4] = unpk(c); }
constexpr u32 R_SUM = 32768, R_PITCH = 2624;
template <int NP, int NR> struct Partials {
    static constexpr int NKB = NR * 5 / 2, NI = (NKB + 3) / 4, LOADS = NI * NP;
    v4u a[NI][NP]; u32 to[NI]; int last;
    DEV Partials(int w, int lane) {
#pragma unroll
        for (int i = 0; i < NI; ++i) {
            const int u = (i * 4 + w) * 64 + lane;
            to[i] = i * 4 + w < NKB ? R_SUM + (u32)((u / 4 % NR) * R_PITCH + (u / (4 * NR)) * 64 + (u & 3) * 16) : R_SUM + NR * R_PITCH + lane * 16;
        }
        last = lane * 16 + ((NI - 1) * 4 + w < NKB ? 0 : OOB);
    }
    DEV void load(rsrc_t ws, int soff, int w, int lane) {
#pragma unroll
        for (int i = 0; i < NI; ++i)
#pragma unroll
            for (int k = 0; k < NP; ++k) a[i][k] = ld16(ws, i == NI - 1 ? last : lane * 16, soff + k * PART + (i * 4 + w) * 1024);
    }
    DEV void sum() const {
#pragma unroll
        for (int i = 0; i < NI; ++i) {
            f32x4 t = __builtin_bit_cast(f32x4, a[i][0]);
#pragma unroll
            for (int k = 1; k < NP; ++k) t += __builtin_bit_cast(f32x4, a[i][k]);
            lds16w(to[i], __builtin_bit_cast(v4u, t));
        }
    }
};
DEV void row_sum(Row& o, int row, int lane) {
    const u32 b = R_SUM + row * R_PITCH;
    const f32x4 a = __builtin_bit_cast(f32x4, lds16(b + lane * 16)), c = __builtin_bit_cast(f32x4, lds16(b + 1024 + lane * 16));
    const f32x2 d = *(const LAS f32x2*)(b + 2048 + lane * 8);
    o.v[0] = (f32x2){a[0], a[1]}; o.v[1] = (f32x2){a[2], a[3]}; o.v[2] = (f32x2){c[0], c[1]}; o.v[3] = (f32x2){c[2], c[3]}; o.v[4] = d;
}
DEV void add_round(Row& x, const Row& o, u32 (&e)[5]) {
#pragma unroll
    for (int i = 0; i < 5; ++i) { e[i] = pk2(x.v[i] + unpk(pk2(o.v[i]))); x.v[i] = unpk(e[i]); }
}
DEV void norm_input(const Params& p, rsrc_t ws, int layer, int cu, int w, int lane) {
    const FragMap<4> fm(cu * 4, w * 64 + lane);
    for (int r = 0; r < (layer ? 14 : 2); ++r) {
        const int s0 = r < 2 ? 8 * r + layer : 4 * r + 8, ds = r < 2 ? 2 : 1;
        v2u a[4], b[4]; u32 c[4];
#pragma unroll
        for (int k = 0; k < 4; ++k) {
            const GAS unsigned char* src = (const GAS unsigned char*)p.x + (size_t)((s0 + ds * k) * 32 + cu * 4 + w) * 1280;
            a[k] = *(const GAS v2u*)(src + lane * 8); b[k] = *(const GAS v2u*)(src + 512 + lane * 8); c[k] = *(const GAS u32*)(src + 1024 + lane * 4);
        }
#pragma unroll
        for (int k = 0; k < 4; k += 2) {
            Row x[2], y[2]; unpack_row(x[0], a[k], b[k], c[k]); unpack_row(x[1], a[k + 1], b[k + 1], c[k + 1]);
            layer_norm2(x, y);
            row_to_lds(y[0], k * 4 + w, lane); row_to_lds(y[1], k * 4 + 4 + w, lane);
        }
        bar();
#pragma unroll
        for (int k = 0; k < 4; ++k) fm.store(ws, OFF_XN + (s0 + ds * k) * 40960, k * 4);
        drain(); bar();
        if (w == 0) set_word(ws, (F_R0 + layer * 8 + cu) * FL_STRIDE, s0 + 3 * ds, lane);
        if (r == 0) MARK(0); else MARK(1);
    }
}
template <int STAGE>
DEV void role_rows(const Params& p, rsrc_t ws, int layer, int cu, int w, int lane) {
    STAMP_DECL;
    constexpr int RW = STAGE ? 2 : 1, NR = 4 * RW, NP = STAGE ? 2 : 4;
    const rsrc_t xin = __builtin_amdgcn_make_buffer_rsrc((void*)p.x, 0, XSZ, 0x00020000), out = __builtin_amdgcn_make_buffer_rsrc((void*)p.out, 0, XSZ, 0x00020000);
    const rsrc_t res = (STAGE == 0 && layer == 0) ? xin : ws;
    const int row0 = (cu * NR + w * RW) * 1280;
    const int res0 = row0 + (STAGE ? OFF_X1 + layer * XSZ : layer ? OFF_X2 : 0), dst0 = row0 + (STAGE == 0 ? OFF_X1 + layer * XSZ : STAGE == 1 ? OFF_X2 : 0);
    const int par0 = (STAGE ? OFF_PM + layer * PM_L : OFF_PO + layer * PO_L) + cu * NR * 2560, nrm0 = STAGE ? OFF_XN + XSZ : OFF_XN1 + layer * XSZ;
    int ps2;
    const int ps = page_sel(ws, ps2);
    const int fo = (STAGE ? F_M2 + layer * 16 + min(lane, 9) : F_O + layer * 20 + min(lane, 19)) * FL_STRIDE + ps;
    const int fme = STAGE ? (F_R2 + cu) * FL_STRIDE + (ps ^ 8192) : (F_R1 + layer * 8 + cu) * FL_STRIDE + ps;
    const int fmo = w ? OOB : STAGE ? (lane < 2 ? lane * 4 * FL_STRIDE : OOB) : (lane ? OOB : 0);
    const FragMap<NR> fm(cu * NR, w * 64 + lane);
    Partials<NP, NR> pa(w, lane);
    v2u a[RW], b[RW]; u32 c[RW];
    auto load = [&](int s) {
        pa.load(ws, par0 + s * NP * PART, w, lane);
#pragma unroll
        for (int k = 0; k < RW; ++k) { const int ro = res0 + s * 40960 + k * 1280;
            a[k] = ld8(res, lane * 8, ro); b[k] = ld8(res, 512 + lane * 8, ro); c[k] = ld4(res, 1024 + lane * 4, ro); }
        PIN();
    };
    wait_flags<0x300 + STAGE>(ws, fo, 0);
    load(0);
    for (int s = 0; s < NSEG; ++s) {
        STAMP2(s, 0);
        pa.sum();
        Row x[RW]; u32 e[RW][5];
#pragma unroll
        for (int k = 0; k < RW; ++k) unpack_row(x[k], a[k], b[k], c[k]);
        cfence(); const int f = (int)ld4(ws, fo, 0); cfence();
        bar();
#pragma unroll
        for (int k = 0; k < RW; ++k) { Row o; row_sum(o, w * RW + k, lane); add_round(x[k], o, e[k]); }
        if constexpr (STAGE < 2) {
            Row y[RW];
            if constexpr (RW == 2) layer_norm2(x, y); else layer_norm(x[0], y[0]);
#pragma unroll
            for (int k = 0; k < RW; ++k) row_to_lds(y[k], w * RW + k, lane);
        }
        const int miss = missing(f, s + 1, NSEG, OOB);
        bar();
        if constexpr (STAGE < 2) fm.store(ws, nrm0 + s * 40960, 0);
#pragma unroll
        for (int k = 0; k < RW; ++k) { if (STAGE < 2) row_store<SC1>(e[k], ws, dst0 + s * 40960 + k * 1280, lane); else row_store<0>(e[k], out, dst0 + s * 40960 + k * 1280, lane); }
        PIN();
        if (!miss) { load(s + 1); waitvm<Partials<NP, NR>::LOADS + 3 * RW>(); } else if (STAGE < 2) drain();
        if constexpr (STAGE < 2) { bar(); st4((u32)s, ws, fmo, fme); }
        STAMP2(s, 2);
        if (miss && s + 1 < NSEG) { wait_flags<0x304 + STAGE>(ws, fo, s + 1); STAMP2(s, 3); load(s + 1); }
    }
    STAMP_DUMP();
}

DEV int qkv_flag(int h, int part, int j) { return (((part == 1 ? 256 : 0) + 8 * h + 7 * j) / 12 + (part == 2 ? F_V : F_QK)) * FL_STRIDE; }

#define ATT_TICKS -12
#define B_TICKS 9
#define HINT_SLEEP 0
DEV void probe_pages(const Params& p, rsrc_t ws, int w, int lane) {
    v4u b = {~0u, ~0u, ~0u, ~0u};
    for (int r = 0; r < PROBES; ++r)
#pragma unroll
        for (int k = 0; k < 4; ++k) {
            const int off = (k < 2 ? OFF_OM : OFF_KVM) + (k & 1) * 8192 + (r * 4 + w) * 16384 + ((r ^ w) & 1) * 4096 + (lane & 7) * 16;
            st16((v4u){~0u, ~0u, ~0u, ~0u}, ws, off, 0);
            drain();
            unsigned long long t0, t1;
            asm volatile("s_memtime %0\n\ts_waitcnt lgkmcnt(0)" : "=s"(t0)::"memory");
            v4u d = ld16(ws, off, 0);
            asm volatile("s_waitcnt vmcnt(0)\n\ts_memtime %0\n\ts_waitcnt lgkmcnt(0)" : "=s"(t1), "+v"(d)::"memory");
            b[k] = min(b[k], (u32)(t1 - t0));
        }
    lds16w((u32)(w * 16), b);
    bar();
#pragma unroll
    for (int k = 0; k < 4; ++k) { const v4u o = lds16((u32)(k * 16)); b = (v4u){min(b.x, o.x), min(b.y, o.y), min(b.z, o.z), min(b.w, o.w)}; }
    const int ps = b.y < b.x ? 8192 : 0;
    if (w == 0) set_word(ws, F_PS * FL_STRIDE, ps | (b.w < b.z ? 1 : 0), lane);
    const int i = lane & 15;
    for (int ks = w; ks < 10; ks += 4) {
        v4u m = *(const GAS v4u*)(p.mem0 + (2 * (i >> 2) + (i & 1)) * DM + 64 * ks + 16 * (lane >> 4) + 8 * ((i >> 1) & 1));
#pragma unroll
        for (int j = 0; j < 4; ++j) m[j] = min(m[j], 0xfffffffeu);
        for (int r = 0; r < 2 * COPIES; ++r) st16(m, ws, ps + mem_kb(r % COPIES, ks) + lane * 16, OFF_MEM + (r / COPIES) * RING * MEM_SLOT);
    }
    bar();
}
template <int CODE, int TICKS> DEV void wait_hint(rsrc_t ws, int voff, int target) {
    u32 cnt = SPIN_CAP; int f, t;
    asm volatile("1:\n\tbuffer_load_dword %1, %3, %4, 0 offen sc1\n\ts_waitcnt vmcnt(0)\n\tv_bfe_i32 %2, %1, 0, 8\n\tv_cmp_gt_i32 vcc, %5, %2\n\ts_cbranch_vccz 2f\n\t"
                 GIVE_UP("%0", "%1", "%4", "%6", "%7")
                 : "+s"(cnt), "=&v"(f), "=&v"(t) : "v"(voff), "s"(ws), "s"(target), "s"(F_ABORT * FL_STRIDE), "n"(CODE) : "vcc", "scc", "memory");
    const u32 now = (u32)__builtin_amdgcn_s_memrealtime();
    int left = (int)((u32)f - (now << 8)) >> 8;
    left = max(left, (int)dppu<0xB1>(left)); left = max(left, (int)dppu<0x4E>(left)); left = max(left, (int)dppu<0x141>(left));
    const u32 until = now + min(__builtin_amdgcn_readfirstlane(left) + TICKS, 300);
    while ((int)((u32)__builtin_amdgcn_s_memrealtime() - until) < 0) { }
}
DEV int kvm_page(int h, int ps2) { return OFF_KVM + ps2 + (h >> 1) * 16384 + (h & 1) * 4096; }
DEV void role_att(const Params& p, rsrc_t ws, int layer, int h, int w, int lane) {
    STAMP_DECL;
    const int g = lane >> 4, t = lane & 15;
    v4u ONES = {~0u, ~0u, ~0u, ~0u};
    opaque(ONES);
    int ps2;
    const int ps = page_sel(ws, ps2);
    const int fq = qkv_flag(h, min(lane >> 1, 2), lane & 1) + layer * F_L * FL_STRIDE + ps, fmo = lane < 2 ? ps ^ (lane * 8192) : OOB;
    const int fa = OFF_HINT + ps + layer * 4096 + h * 128, mb0 = kvm_page(h, ps2) + layer * KVM_RING * KVM_SLOT;
    for (int i = 0; i < KVM_RING * 4; ++i) st16(ONES, ws, lane * 16 + (i & 3) * 1024, mb0 + (i >> 2) * KVM_SLOT);
    drain();
    set_word(ws, (F_ATT + layer * F_L + h) * FL_STRIDE + ps, KVM_RING - 1, lane);
    MARK(0);
    const int kmo = (g * 8 + 2 * (t >> 2) + (t & 1)) * 16, oo = h * 8192 + (g & 1) * 1024 + ((g >> 1) * 16 + t) * 16;
    constexpr float SCALE = 0.08838834764831845f, LOG2E = 1.4426950408889634f;
    v4u Q[4][2], K[4][2], V[8];
    auto fetch = [&](int part, int i, int voff, int s) { return ld16(ws, voff + (i & 3) * 1024, OFF_QKV + layer * QKV_L + h * 24576 + s * QKV_SEG + part * 8192 + (i >> 2) * 4096); };
    auto sc2 = [&](float a, float b, float& oa, float& ob) { const u32 d = pk2(a, b); const u32 e = pk2(bflo(d) * SCALE, bfhi(d) * SCALE); oa = bflo(e); ob = bfhi(e); };
    float x0[4], x1[8], m0, m1, l0, l1;
    auto own_scores = [&]() {
        f32x4 s00, s01, s11;
        mfv0(s00, K[0][0], Q[0][0]); mfv0(s01, K[0][0], Q[0][1]); mfv0(s11, K[0][1], Q[0][1]);
#pragma unroll
        for (int k = 1; k < 4; ++k) { mfv(s00, K[k][0], Q[k][0]); mfv(s01, K[k][0], Q[k][1]); mfv(s11, K[k][1], Q[k][1]); }
        settle3(s00, s01, s11);
        sc2(s00[0], s00[1], x0[0], x0[1]); sc2(s00[2], s00[3], x0[2], x0[3]);
        sc2(s01[0], s01[1], x1[0], x1[1]); sc2(s01[2], s01[3], x1[2], x1[3]); sc2(s11[0], s11[1], x1[4], x1[5]); sc2(s11[2], s11[3], x1[6], x1[7]);
#pragma unroll
        for (int r = 0; r < 4; ++r) { const bool vis = 4 * g + r <= t; x0[r] = vis ? x0[r] : -1e9f; x1[4 + r] = vis ? x1[4 + r] : -1e9f; }
        m0 = xrow_max(vmax2(vmax3(x0[0], x0[1], x0[2]), x0[3]));
        m1 = xrow_max(vmax3(vmax3(x1[0], x1[1], x1[2]), vmax3(x1[3], x1[4], x1[5]), vmax2(x1[6], x1[7])));
        asm volatile("" : "+v"(m0), "+v"(m1));
    };
    auto own_exps = [&]() {
        l0 = 0; l1 = 0;
#pragma unroll
        for (int i = 0; i < 4; ++i) { x0[i] = __builtin_amdgcn_exp2f((x0[i] - m0) * LOG2E); l0 += x0[i]; }
#pragma unroll
        for (int i = 0; i < 8; ++i) { x1[i] = __builtin_amdgcn_exp2f((x1[i] - m1) * LOG2E); l1 += x1[i]; }
        asm volatile("" : "+v"(l0), "+v"(l1), "+v"(x0[0]), "+v"(x0[1]), "+v"(x0[2]), "+v"(x0[3]), "+v"(x1[0]), "+v"(x1[1]), "+v"(x1[2]), "+v"(x1[3]),
                     "+v"(x1[4]), "+v"(x1[5]), "+v"(x1[6]), "+v"(x1[7]));
    };
    wait_flags<0x400>(ws, fq, 0);
#pragma unroll
    for (int i = 0; i < 8; ++i) { K[i >> 1][i & 1] = fetch(1, i, lane * 16, 0); Q[i >> 1][i & 1] = fetch(0, i, lane * 16, 0); }
#pragma unroll
    for (int i = 0; i < 8; ++i) V[i] = fetch(2, i, lane * 16, 0);
    int fn = (int)ld4(ws, fq, 0);
    own_scores(); own_exps();
    for (int s = 0; s < NSEG; ++s) {
        f32x4 sm0, sm1;
        const int miss = missing(fn, s + 1, NSEG, OOB), nx = lane * 16 + miss;
        wait_hint<0x401, ATT_TICKS>(ws, fa, s);
        const int mb = mb0 + (s & (KVM_RING - 1)) * KVM_SLOT;
        v4u KM[4], Qn[4]; u32 VM[8];
        auto rows = [&]() {
#pragma unroll
            for (int k = 0; k < 4; ++k) KM[k] = ld16(ws, kmo + k * 512, mb);
#pragma unroll
            for (int e = 0; e < 4; ++e) { const v2u t2 = ld8(ws, 2048 + e * 512 + lane * 8, mb); VM[2 * e] = t2.x; VM[2 * e + 1] = t2.y; }
            PIN();
        };
        auto empty = [&]() {
            u32 mx = 0;
#pragma unroll
            for (int k = 0; k < 4; ++k) mx = umax3(vmax4(mx, KM[k]), VM[2 * k], VM[2 * k + 1]);
            return __any(mx == 0xffffffffu);
        };
        rows();
#pragma unroll
        for (int i = 0; i < 8; ++i) K[i >> 1][i & 1] = fetch(1, i, nx, s + 1);
#pragma unroll
        for (int i = 0; i < 4; ++i) Qn[i] = fetch(0, i, nx, s + 1);
        PIN();
        if (__builtin_expect(empty(), 0)) {
            u32 spins = SPIN_CAP;
            do { spin_check<0x403>(spins, ws); rows(); } while (empty());
        }
        STAMP2(s, 0);
        mfv0(sm0, KM[0], Q[0][0]); mfv0(sm1, KM[0], Q[0][1]);
#pragma unroll
        for (int k = 1; k < 4; ++k) { mfv(sm0, KM[k], Q[k][0]); mfv(sm1, KM[k], Q[k][1]); }
#pragma unroll
        for (int i = 4; i < 8; ++i) Q[i >> 1][i & 1] = fetch(0, i, nx, s + 1);
        PAD(3); asm volatile("" : "+v"(sm0), "+v"(sm1));
        float y0[2], y1[2];
        sc2(sm0[0], sm0[1], y0[0], y0[1]); sc2(sm1[0], sm1[1], y1[0], y1[1]);
        const float n0 = xrow_max(vmax3(y0[0], y0[1], m0)), n1 = xrow_max(vmax3(y1[0], y1[1], m1));
        const float f0 = __builtin_amdgcn_exp2f((m0 - n0) * LOG2E), f1 = __builtin_amdgcn_exp2f((m1 - n1) * LOG2E);
#pragma unroll
        for (int i = 0; i < 2; ++i) { y0[i] = __builtin_amdgcn_exp2f((y0[i] - n0) * LOG2E); y1[i] = __builtin_amdgcn_exp2f((y1[i] - n1) * LOG2E); }
        const float i0 = __builtin_amdgcn_rcpf(xrow_sum(l0 * f0 + (y0[0] + y0[1]))), i1 = __builtin_amdgcn_rcpf(xrow_sum(l1 * f1 + (y1[0] + y1[1])));
        const float j0 = i0 * f0, j1 = i1 * f1;
        v4u P0 = {pk2(y0[0] * i0, y0[1] * i0), 0u, pk2(x0[0] * j0, x0[1] * j0), pk2(x0[2] * j0, x0[3] * j0)};
        v4u P1m = {pk2(y1[0] * i1, y1[1] * i1), 0u, 0u, 0u};
        v4u P1 = {pk2(x1[0] * j1, x1[1] * j1), pk2(x1[2] * j1, x1[3] * j1), pk2(x1[4] * j1, x1[5] * j1), pk2(x1[6] * j1, x1[7] * j1)};
        v4u vmx[8];
#pragma unroll
        for (int e = 0; e < 8; ++e) vmx[e] = (v4u){VM[e], 0u, V[e].x, V[e].y};
        asm volatile("s_nop 1" : "+v"(P0), "+v"(P1m), "+v"(P1), "+v"(vmx[0]), "+v"(vmx[1]), "+v"(vmx[2]), "+v"(vmx[3]), "+v"(vmx[4]), "+v"(vmx[5]), "+v"(vmx[6]), "+v"(vmx[7]));
        f32x4 o0[8], o1[8];
        const int ob = OFF_OB + layer * OB_L + s * OB_SEG;
#pragma unroll
        for (int e = 0; e < 10; ++e) {
            if (e < 8) { mfv0(o0[e], vmx[e], P0); mfv0(o1[e], V[e], P1); mfv(o1[e], vmx[e], P1m); }
            if (e >= 2) {
                const int d = e - 2;
                if (e < 8) PAD(1); else PAD(7);
                asm volatile("" : "+v"(o0[d]), "+v"(o1[d]));
                st16(pack_swap(pk2(o0[d][0], o0[d][1]), pk2(o0[d][2], o0[d][3]), pk2(o1[d][0], o1[d][1]), pk2(o1[d][2], o1[d][3])), ws,
                     oo + (d & 1) * 512 + ((d >> 1) & 1) * 2048, ob + (d >> 2) * 4096);
            }
            PIN();
        }
#pragma unroll
        for (int i = 0; i < 4; ++i) st16(ONES, ws, lane * 16 + i * 1024, mb);
        PIN();
#pragma unroll
        for (int i = 0; i < 4; ++i) Q[i >> 1][i & 1] = Qn[i];
        asm volatile("" : "+v"(Q[0][0]), "+v"(Q[0][1]), "+v"(Q[1][0]), "+v"(Q[1][1]), "+v"(Q[2][0]), "+v"(Q[2][1]), "+v"(Q[3][0]), "+v"(Q[3][1]));
#pragma unroll
        for (int i = 0; i < 8; ++i) V[i] = fetch(2, i, nx, s + 1);
        cfence(); fn = (int)ld4(ws, fq, 0); cfence();
        own_scores();
        waitvm<9>();
        st4((u32)(s + (lane ? 0 : KVM_RING)), ws, fmo, (F_ATT + layer * F_L + h) * FL_STRIDE);
        STAMP2(s, 2);
        if (s + 1 == NSEG) break;
        if (miss) {
            wait_flags<0x402>(ws, fq, s + 1);
#pragma unroll
            for (int i = 0; i < 8; ++i) { K[i >> 1][i & 1] = fetch(1, i, lane * 16, s + 1); Q[i >> 1][i & 1] = fetch(0, i, lane * 16, s + 1); }
#pragma unroll
            for (int i = 0; i < 8; ++i) V[i] = fetch(2, i, lane * 16, s + 1);
            cfence(); fn = (int)ld4(ws, fq, 0); cfence();
            own_scores();
        }
        own_exps();
    }
    STAMP_DUMP();
}

DEV void next_step(int& c, int& s) { next_item<DELTA + 1>(c, s); }

DEV void role_b(const Params& p, rsrc_t ws, int tg, int cg, int w, int lane) {
    STAMP_DECL;
    v8u W[2][16];
    {
        const int gb = lane >> 4, q = (lane >> 2) & 3, pp = lane & 3;
        const int prow = ((lane >> 2) & 9) | ((lane >> 1) & 4) | ((lane >> 3) & 2);
        auto issue = [&](int c) {
            for (int i = 0; i < 16; ++i)
                glds16(p.Wo + (size_t)(w * 1024 + c * 256 + i * 16 + prow) * DM + cg * 32 + ((lane & 3) ^ ((i & 1) << 1)) * 8, (u32)((w * 2 + (c & 1)) * 16384 + i * 1024));
        };
        issue(0);
        const u32 rd0 = (u32)(w * 32768 + (32 * (gb >> 1) + 16 * (gb & 1) + q) * 64 + pp * 8);
        const u32 rd[2] = {rd0 + (gb & 1) * 32, rd0 + ((gb & 1) ^ 1) * 32};
#pragma unroll
        for (int c = 0; c < 4; ++c) {
            if (c < 3) { issue(c + 1); waitvm<16>(); } else drain();
#pragma unroll
            for (int k = 0; k < 4; ++k)
#pragma unroll
                for (int t = 0; t < 2; ++t) {
                    const u32 a = rd[t] + (c & 1) * 16384 + ((k >> 1) * 128 + 8 * (k & 1)) * 64;
                    const v2u r0 = ldtr(a), r1 = ldtr(a + 4 * 64), r2 = ldtr(a + 64 * 64), r3 = ldtr(a + 68 * 64);
                    W[t][c * 4 + k] = (v8u){r0.x, r0.y, r1.x, r1.y, r2.x, r2.y, r3.x, r3.y};
                }
            asm volatile("s_waitcnt lgkmcnt(0)" ::: "memory");
        }
    }
    bar();
    MARK(0);
    const int idx = (lane & 4) ? 0xEEEE : 0x4444;
    const int gd = lane >> 4, n = lane & 15;
    const v4u ONES = {~0u, ~0u, ~0u, ~0u};
    const int u = lane >> 2;
    int ps2;
    const int ps = page_sel(ws, ps2);
    const int ooff = (lane & 3) < COPIES ? ps + mem_kb(lane & 3, cg >> 1) + ((2 * (cg & 1) + (u >> 3)) * 16 + 8 * tg + (u & 7)) * 16 : OOB;
    const int utok = 2 * ((u >> 2) & 1) + (u & 1), uhf = (u >> 1) & 1;
    const u32 rrd = (u32)((utok * 32 + (u >> 3) * 16 + uhf * 8 + (lane & 3) * 2) * 4);
    const int hoff = OFF_HINT + ps + (w * 8 + (lane & 7)) * 128;
    u32 redw = (u32)(w * 512 + (((gd & 1) * 32 + (gd >> 1) * 16 + n) * 4)), redr = rrd;
    auto empty = [](const v4u& d) {
        u32 m; unsigned long long e;
        asm volatile("v_max3_u32 %0, %2, %3, %4\n\tv_max_u32 %0, %0, %5\n\tv_cmp_eq_u32 %1, -1, %0" : "=&v"(m), "=s"(e) : "v"(d.x), "v"(d.y), "v"(d.z), "v"(d.w));
        return e;
    };
    int c = 0, s = 0;
    for (int it = 0; it < 2 * NSEG; ++it, next_step(c, s)) {
        if (s == NSEG - 1) continue;
        int src = OFF_OM + (c * RING + (s & (RING - 1))) * OM_SLOT + (tg * 4 + w) * 16384 + ps, src2 = src + 4096;
        int dst = OFF_MEM + (c * RING + ((s + 1) & (RING - 1))) * MEM_SLOT, rst = OFF_MEM + (c * RING + (s & (RING - 1))) * MEM_SLOT;
        f32x4 a00 = {0, 0, 0, 0}, a01 = a00, a10 = a00, a11 = a00;
        opaque_s(src); opaque_s(src2); opaque_s(dst); opaque_s(rst);
        asm volatile("" : "+v"(a00), "+v"(a01), "+v"(a10), "+v"(a11));
        wait_hint<0x501, B_TICKS>(ws, hoff + c * 4096, s);
        v4u d[8];
#pragma unroll
        for (int i = 0; i < 8; ++i) { d[i] = ld16(ws, lane * 16 + (i & 3) * 1024, i < 4 ? src : src2); PIN(); }
        unsigned long long miss = empty(d[0]);
#pragma unroll
        for (int i = 0; i < 8; ++i) {
            if (__builtin_expect(miss != 0, 0)) {
                u32 spins = SPIN_CAP;
                do {
                    spin_check<0x500>(spins, ws);
#pragma unroll
                    for (int j = i; j < 8; ++j) { d[j] = ld16(ws, lane * 16 + (j & 3) * 1024, j < 4 ? src : src2); PIN(); }
                } while (empty(d[i]) != 0);
            }
            smfa(a00, d[i], W[0][2 * i], idx); smfa(a01, d[i], W[0][2 * i + 1], idx); smfa(a10, d[i], W[1][2 * i], idx);
            if (i < 7) miss = empty(d[i + 1]);
            smfa(a11, d[i], W[1][2 * i + 1], idx);
            PIN();
        }
        settle4(a00, a01, a10, a11);
        STAMP2(it, 0);
        float y[4];
#pragma unroll
        for (int r = 0; r < 4; ++r) {
            auto p0 = __builtin_amdgcn_permlane32_swap(__float_as_uint(a00[r]), __float_as_uint(a10[r]), false, false);
            auto p1 = __builtin_amdgcn_permlane32_swap(__float_as_uint(a01[r]), __float_as_uint(a11[r]), false, false);
            y[r] = __uint_as_float(p0[0]) + __uint_as_float(p1[1]);
        }
        auto z0 = __builtin_amdgcn_permlane16_swap(__float_as_uint(y[0]), __float_as_uint(y[1]), false, false);
        auto z1 = __builtin_amdgcn_permlane16_swap(__float_as_uint(y[2]), __float_as_uint(y[3]), false, false);
        *(LAS float*)redw = __uint_as_float(z0[0]) + __uint_as_float(z0[1]);
        *(LAS float*)(redw + 256) = __uint_as_float(z1[0]) + __uint_as_float(z1[1]);
        bar();
        if (w == 0) {
            const f32x2 t0 = *(const LAS f32x2*)redr, t1 = *(const LAS f32x2*)(redr + 512), t2 = *(const LAS f32x2*)(redr + 1024), t3 = *(const LAS f32x2*)(redr + 1536);
            const f32x2 acc = (t0 + t1) + (t2 + t3);
            const u32 x = min(pk2(acc.x, acc.y), 0xfffffffeu);
            st16((v4u){dppu<0x00>(x), dppu<0x55>(x), dppu<0xAA>(x), dppu<0xFF>(x)}, ws, ooff, dst);
            st16(ONES, ws, ooff, rst);
        }
        redw ^= 2048; redr ^= 2048; opaque(redw); opaque(redr);
        STAMP2(it, 2);
    }
    STAMP_DUMP();
}

constexpr u32 A_WV = 0, A_MEM = 139264, A_IMG = A_MEM + 10240, A_KX = A_IMG + 4096;
constexpr u32 A_SINK = A_KX + 8192;
static_assert(A_SINK + 1024 <= LDS_BYTES, "role A LDS");
DEV void ask_rows0(v4u& g, rsrc_t ws, int off, int src) { asm volatile("buffer_load_dwordx4 v[228:231], %1, %2, %3 offen sc1" : "={v[228:231]}"(g) : "v"(off), "s"(ws), "s"(src) : "memory"); }
DEV void ask_rows1(v4u& g, rsrc_t ws, int off, int src) { asm volatile("buffer_load_dwordx4 v[232:235], %1, %2, %3 offen sc1" : "={v[232:235]}"(g) : "v"(off), "s"(ws), "s"(src) : "memory"); }
DEV void ask_rows2(v4u& g, rsrc_t ws, int off, int src) { asm volatile("buffer_load_dwordx4 v[236:239], %1, %2, %3 offen sc1" : "={v[236:239]}"(g) : "v"(off), "s"(ws), "s"(src) : "memory"); }
DEV void rows_check(rsrc_t ws, int o0, int o1, int o2, int src, u32 from, v4u& g0, v4u& g1, v4u& g2) {
    u32 cnt = SPIN_CAP, m; unsigned long long now;
#define ROWS_MAX(n2, n1, n0) "s_waitcnt vmcnt(" n2 ")\n\tv_max3_u32 %4, v228, v229, v230\n\ts_waitcnt vmcnt(" n1 ")\n\tv_max3_u32 %4, %4, v231, v232\n\tv_max3_u32 %4, %4, v233, v234\n\t" \
                 "s_waitcnt vmcnt(" n0 ")\n\tv_max3_u32 %4, %4, v235, v236\n\tv_max3_u32 %4, %4, v237, v238\n\tv_max_u32 %4, %4, v239\n\tv_cmp_eq_u32 vcc, -1, %4\n\ts_cbranch_vccz 2f\n"
    asm volatile(ROWS_MAX("3", "2", "1")
                 "4:\n\ts_memrealtime s[98:99]\n\ts_waitcnt lgkmcnt(0)\n\ts_sub_u32 s98, s98, %12\n\ts_cmp_lt_i32 s98, 0\n\ts_cbranch_scc1 4b\n"
                 "1:\n\tbuffer_load_dwordx4 v[228:231], %6, %9, %10 offen sc1\n\t"
                 "buffer_load_dwordx4 v[232:235], %7, %9, %10 offen sc1\n\tbuffer_load_dwordx4 v[236:239], %8, %9, %10 offen sc1\n\t"
                 ROWS_MAX("2", "1", "0") GIVE_UP("%3", "%4", "%9", "%11", "0x601")
                 : "+{v[228:231]}"(g0), "+{v[232:235]}"(g1), "+{v[236:239]}"(g2), "+s"(cnt), "=&v"(m), "=&{s[98:99]}"(now)
                 : "v"(o0), "v"(o1), "v"(o2), "s"(ws), "s"(src), "s"(F_ABORT * FL_STRIDE), "s"(from) : "vcc", "scc", "memory");
}
__host__ __device__ constexpr int vfrag(int ks, int t) { return (t == 0 && ks % 3 == 2) ? -1 : 2 * ks + t - (ks + 1) / 3; }
DEV void role_a(const Params& p, rsrc_t ws, int h, int copy, int w, int lane) {
    STAMP_DECL; SACC_DECL;
    v8u WQ[2][10], WK[2][10], WV[3];
    const int g = lane >> 4, c16 = lane & 15;
    {
        const int q = (lane >> 2) & 3, pp = lane & 3;
        auto issue = [&](int c) {
            const u16* M = c < 2 ? p.Wq : c < 4 ? p.Wk : p.Wv;
            for (int i = 0; i < 20; ++i)
                glds16(M + (size_t)((c & 1) * 320 + (i * 4 + w) * 4 + g) * HD + h * 128 + (c16 ^ (((g & 1) | w << 1) << 1)) * 8, (u32)((c & 1) * 81920 + (i * 4 + w) * 1024));
        };
        issue(0);
        const int sw = (q & 1) | (g & 1) << 1 | (q >> 1) << 2;
        const u32 rd0 = (u32)((16 * (g >> 1) + 8 * (q >> 1) + 4 * (g & 1) + (q & 1)) * 256 + pp * 8);
        const u32 rd[2] = {rd0 + ((2 * w) ^ sw) * 32, rd0 + ((2 * w + 1) ^ sw) * 32};
#pragma unroll
        for (int c = 0; c < 6; ++c) {
            if (c < 5) { issue(c + 1); waitvm<20>(); } else drain();
            bar();
            if (c == 1 || c == 3) MARK(1 + (c >> 1));
            v8u tmp[2][5];
#pragma unroll
            for (int k = 0; k < 5; ++k)
#pragma unroll
                for (int t = 0; t < 2; ++t) {
                    const u32 a = rd[t] + (c & 1) * 81920 + k * 64 * 256;
                    const v2u r0 = ldtr(a), r1 = ldtr(a + 2 * 256), r2 = ldtr(a + 32 * 256), r3 = ldtr(a + 34 * 256);
                    const v8u f = {r0.x, r0.y, r1.x, r1.y, r2.x, r2.y, r3.x, r3.y};
                    if (c < 2) WQ[t][c * 5 + k] = f; else if (c < 4) WK[t][(c - 2) * 5 + k] = f; else tmp[t][k] = f;
                }
            bar();
            if (c >= 4) {
#pragma unroll
                for (int k = 0; k < 5; ++k)
#pragma unroll
                    for (int t = 0; t < 2; ++t) {
                        const int ks = (c - 4) * 5 + k, f = vfrag(ks, t);
                        if (f >= 0) {
                            const u32 a = A_WV + (u32)(f * 8192 + w * 2048 + lane * 16);
                            lds16w(a, (v4u){tmp[t][k][0], tmp[t][k][1], tmp[t][k][2], tmp[t][k][3]});
                            lds16w(a + 1024, (v4u){tmp[t][k][4], tmp[t][k][5], tmp[t][k][6], tmp[t][k][7]});
                        } else WV[ks / 3] = tmp[t][k];
                    }
            }
        }
    }
    MARK(0);
    u32 bw0 = (u32)(w * 2048 + lane * 16), bw1 = bw0 + 65536, bw2 = bw0 + 131072, bl = (u32)(131072 + lane * 16);
    u32 blw = bl + w * 1024;
    opaque(bw0); opaque(bw1); opaque(bw2); opaque(bl); opaque(blw);
    const int idx = (lane & 2) ? 0xEEEE : 0x4444;
    const v4u ONES = {~0u, ~0u, ~0u, ~0u};
    int ps2;
    const int ps = page_sel(ws, ps2);
    const bool qlane = (c16 & 2) != 0;
    const int tok = 2 * (c16 >> 2) + (c16 & 1);
    const int omoff = qlane ? ps + ((tok >> 2) * 4 + (h >> 3)) * 16384 + (h & 7) * 1024 + (w * 16 + g * 4 + (tok & 3)) * 16 : OOB;
    const int kmoff = qlane ? OOB : w * 512 + (g * 8 + tok) * 16;
    const int hintoff = (w == 0 && lane == 0) ? OFF_HINT + ps + h * 128 : OOB;
    const int gmo0 = ps + lane * 16 + mem_kb(copy, w), gmo1 = ps + lane * 16 + mem_kb(copy, 4 + w), gmo2 = ps + lane * 16 + mem_kb(copy, 8 + (w & 1));
    const int kvo = lane * 16 + w * 2048;
    const int vmoff = 2048 + w * 512 + lane * 8;
    u32 blw2 = w < 2 ? blw + (A_MEM - 131072) + 8192 : A_SINK + lane * 16;
    opaque(blw2);
    const u32 blk = bw2 + (A_KX - 131072);
    u32 imgw[2];
#pragma unroll
    for (int t = 0; t < 2; ++t) { const int e = 16 * t + c16; const int rho = 16 * (e >> 4) + 8 * ((e >> 2) & 1) + 4 * ((e >> 3) & 1) + (e & 3);
        imgw[t] = A_IMG + (u32)((32 * w + rho) * 32 + ((g ^ ((rho >> 2) & 3)) * 8)); }
    u32 imgr[2];
#pragma unroll
    for (int uu = 0; uu < 2; ++uu) { const int rho = 16 * (g >> 1) + 8 * uu + 4 * (g & 1) + ((lane >> 2) & 3);
        imgr[uu] = A_IMG + (u32)(rho * 32 + (((lane & 3) ^ ((rho >> 2) & 3)) * 8)); opaque(imgr[uu]); }
    u32 imgo[2] = {imgr[0] + w * 1024, imgr[1] + w * 1024};
    opaque(imgo[0]); opaque(imgo[1]);
    const int fk0 = (lane < 4 ? qkv_flag(h, 1 + (lane >> 1), lane & 1) : (F_ATT + h) * FL_STRIDE) + ps;
    v4u pk[2], pv[2], gm[3];
    wait_flags<0x600>(ws, fk0, 0);
#pragma unroll
    for (int i = 0; i < 2; ++i) pk[i] = ld16(ws, kvo + i * 1024, OFF_QKV + h * 24576 + 8192);
    asm volatile("" : "+v"(pk[0]), "+v"(pk[1]));
    MARK(3);
    ask_rows0(gm[0], ws, gmo0, OFF_MEM); ask_rows1(gm[1], ws, gmo1, OFF_MEM); ask_rows2(gm[2], ws, gmo2, OFF_MEM);
    drain();
    int took = 110;
    int before = (int)__builtin_amdgcn_s_memrealtime(); unsigned long long stored = (u32)before + 110;
    rows_check(ws, gmo0, gmo1, gmo2, OFF_MEM, before, gm[0], gm[1], gm[2]);
    bar();
#pragma unroll
    for (int i = 0; i < 2; ++i) lds16w(blk + i * 1024, pk[i]);
    v4u ONE1 = {0x3f803f80u, 0x3f803f80u, 0x3f803f80u, 0x3f803f80u};
    opaque(ONE1);
    v4u v0 = {0u, 0u, 0u, 0u}, v1 = v0, Pm = v0;
    f32x4 aq[2], ak[2], av[2];
#define CLEAR_ACC() do { for (int t = 0; t < 2; ++t) { aq[t] = (f32x4){0, 0, 0, 0}; ak[t] = aq[t]; av[t] = aq[t]; } \
        asm volatile("" : "+v"(aq[0]), "+v"(aq[1]), "+v"(ak[0]), "+v"(ak[1]), "+v"(av[0]), "+v"(av[1])); } while (0)
    CLEAR_ACC();
    int t_s, t_ns, t_pv, t_pk, t_om, t_rs, t_rows, t_mb, t_fl;
    auto tables = [&](int hf) {
        auto chain_of = [](int i) { return i <= DELTA ? 0 : i >= 2 * NSEG - DELTA ? 1 : (i - DELTA) & 1; };
        auto seg_of = [](int i) { return i <= DELTA ? i : i >= 2 * NSEG - DELTA ? i - NSEG : ((i - DELTA) & 1) ? (i - DELTA) >> 1 : (i + DELTA) >> 1; };
        int i = hf * 64 + lane; opaque(i);
        const int c = chain_of(i), s = seg_of(i), nc = chain_of(i + 1), ns = seg_of(i + 1);
        t_s = s | c << 20;
        t_ns = ns < NSEG ? ns : -1;
        t_pv = OFF_QKV + c * QKV_L + s * QKV_SEG + h * 24576 + 16384;
        t_pk = OFF_QKV + nc * QKV_L + ns * QKV_SEG + h * 24576 + 8192;
        t_om = OFF_OM + (c * RING + (s & (RING - 1))) * OM_SLOT;
        t_rs = OFF_OM + (c * RING + ((s - 1) & (RING - 1))) * OM_SLOT;
        t_rows = OFF_MEM + (nc * RING + (ns & (RING - 1))) * MEM_SLOT;
        t_mb = kvm_page(h, ps2) + (c * KVM_RING + (s & (KVM_RING - 1))) * KVM_SLOT;
        t_fl = nc * F_L * FL_STRIDE;
    };
    tables(0);
    auto rdwv = [&](int ks, int t, int hf) -> v4u {
        const int f = vfrag(ks, t);
        return lds16((f < 8 ? bw0 : f < 16 ? bw1 : bw2) + (f & 7) * 8192 + hf * 1024);
    };
    auto sethf = [](v8u& d, int hf, v4u v) { d[4 * hf] = v.x; d[4 * hf + 1] = v.y; d[4 * hf + 2] = v.z; d[4 * hf + 3] = v.w; };
    auto rdam = [&](int ks) { return lds16(bl + (A_MEM - 131072) + ks * 1024); };
    for (int it = 0;;) {
        STAMPA(it, 0); SACC_AT(0);
        lds16w(blw + (A_MEM - 131072), gm[0]); lds16w(blw + (A_MEM - 131072) + 4096, gm[1]); lds16w(blw2, gm[2]);
        auto lane_of = [&](int t) { int r = __builtin_amdgcn_readlane(t, it); opaque_s(r); return r; };
        PIN();
        const int fsrc = lane_of(t_fl), pfv = lane_of(t_pv);
        unsigned long long now;
        asm volatile("s_memrealtime %0\n\ts_waitcnt lgkmcnt(0)\n\ts_barrier" : "=s"(now)::"memory");
        v4u KX[4][2], am[3]; v8u wv[2][2];
        am[0] = rdam(0); am[1] = rdam(1);
#pragma unroll
        for (int j = 0; j < 4; ++j) sethf(wv[0][j >> 1], j & 1, rdwv(0, j >> 1, j & 1));
        int ns, pfb, omb, rsb, gsrc, mb, hb, fl, late, pfo, hw, sv, from, due;
        auto book = [&](int i) {
            switch (i) {
                case 0: sv = lane_of(t_s); break;
                case 1: rsb = lane_of(t_rs); break;
                case 2: fl = (int)ld4(ws, fk0, fsrc); break;
                case 5: pv[0] = ld16(ws, kvo, pfv); break;
                case 6: hb = s_shri(sv, 8); break;
                case 7: sv = s_andi(sv, 255); break;
                case 8: pv[1] = ld16(ws, kvo + 1024, pfv); break;
                case 3: asm volatile("s_sub_i32 %0, s96, %2" : "=s"(due) : "{s[96:97]}"(stored), "s"(before) : "scc"); break;
                case 4: asm volatile("s_min_u32 %0, %0, %1" : "+s"(took) : "s"(due) : "scc"); break;
                case 9: due = s_add(took, (int)now); break;
                case 10: took = s_addi(took, 1); break;
                case 11: hw = s_shli(due, 8); break;
                case 12: hw = s_add(hw, sv); break;
                case 13: omb = lane_of(t_om); break;
                case 15: __builtin_amdgcn_raw_buffer_store_b128(ONES, ws, omoff, rsb, SC1); break;
                case 18: gsrc = lane_of(t_rows); break;
                case 19: mb = lane_of(t_mb); break;
                case 23: st4((u32)hw, ws, hintoff, hb); break;
                case 24: pfb = lane_of(t_pk); break;
                case 25: ns = lane_of(t_ns); break;
                case 30: from = s_shli(hb, 3); break;
                case 31: asm volatile("s_cmp_eq_u32 %0, %1\n\ts_cselect_b32 %0, %2, %3" : "+s"(from) : "s"(fsrc), "s"(due), "s"((u32)stored) : "scc"); break;
                case 35: from = s_addi(from, A_TICKS); break;
                case 48:
                    asm volatile("v_cmp_gt_i32 vcc, %1, %2\n\ts_cmp_lg_u64 vcc, 0\n\ts_cselect_b32 %0, %3, 0" : "=&s"(late) : "s"(ns), "v"(fl), "s"(OOB) : "vcc", "scc"); break;
                case 53: pfo = kvo + late; opaque(pfo); break;
                case 54: pk[0] = ld16(ws, pfo, pfb); break;
                case 57: pk[1] = ld16(ws, pfo + 1024, pfb); break;
                default: break;
            }
        };
        PIN();
#pragma unroll
        for (int ks = 0; ks < 10; ++ks) {
            const bool nx = ks < 9, n0 = nx && vfrag(ks + 1, 0) >= 0, kx = ks == 1 || ks == 4 || ks == 7 || ks == 9;
            const int kk = ks == 9 ? 3 : ks / 3;
            smfa(aq[0], am[ks % 3], WQ[0][ks], idx);
            if (ks < 8) am[(ks + 2) % 3] = rdam(ks + 2);
            book(6 * ks); PIN();
            smfa(aq[1], am[ks % 3], WQ[1][ks], idx);
            if (n0) sethf(wv[(ks + 1) & 1][0], 0, rdwv(ks + 1, 0, 0)); else if (kx) KX[kk][0] = lds16(bl + (A_KX - 131072) + (2 * kk) * 1024);
            book(6 * ks + 1); PIN();
            if (ks < 6) smfa(ak[0], am[ks % 3], WK[0][ks], idx); else smfv(ak[0], am[ks % 3], WK[0][ks], idx);
            if (n0) sethf(wv[(ks + 1) & 1][0], 1, rdwv(ks + 1, 0, 1)); else if (kx) KX[kk][1] = lds16(bl + (A_KX - 131072) + (2 * kk + 1) * 1024);
            book(6 * ks + 2); PIN();
            if (ks < 6) smfa(ak[1], am[ks % 3], WK[1][ks], idx); else smfv(ak[1], am[ks % 3], WK[1][ks], idx);
            if (nx) sethf(wv[(ks + 1) & 1][1], 0, rdwv(ks + 1, 1, 0));
            book(6 * ks + 3); PIN();
            if (vfrag(ks, 0) >= 0) smfv(av[0], am[ks % 3], wv[ks & 1][0], idx); else smfv(av[0], am[ks % 3], WV[ks / 3], idx);
            if (nx) sethf(wv[(ks + 1) & 1][1], 1, rdwv(ks + 1, 1, 1));
            book(6 * ks + 4); PIN();
            smfv(av[1], am[ks % 3], wv[ks & 1][1], idx);
            book(6 * ks + 5); PIN();
        }
        PAD(5); asm volatile("" : "+v"(aq[0]), "+v"(aq[1]), "+v"(ak[0]), "+v"(ak[1]));
        auto fold = [](const f32x4& a) { const f32x2 t = (f32x2){a[0], a[1]} + (f32x2){a[2], a[3]}; return pk2(t.x, t.y); };
#pragma unroll
        for (int t = 0; t < 2; ++t) *(LAS v2u*)imgw[t] = (v2u){fold(ak[t]), fold(aq[t])};
        PIN();
        asm volatile("" : "+v"(av[0]), "+v"(av[1]));
        v0.x = fold(av[0]); v1.x = fold(av[1]);
        asm volatile("" : "+v"(v0), "+v"(v1));
        bar();
        v4u X[4], xw;
#pragma unroll
        for (int k = 0; k < 4; ++k) { const v2u lo = ldtr(imgr[0] + k * 1024), hi = ldtr(imgr[1] + k * 1024); X[k] = (v4u){lo.x, lo.y, hi.x, hi.y}; PIN(); }
        { const v2u lo = ldtr(imgo[0]), hi = ldtr(imgo[1]); xw = (v4u){lo.x, lo.y, hi.x, hi.y}; }
        asm volatile("" : "+v"(pv[0]), "+v"(pv[1]));
        __builtin_amdgcn_raw_buffer_store_b64((v2u){v0.x, v1.x}, ws, vmoff, mb, SC1);
        PIN();
        f32x4 sm, s0, s1;
#pragma unroll
        for (int k = 0; k < 4; ++k) {
            if (k == 0) mfv0(sm, X[0], X[0]); else mfv(sm, X[k], X[k]);
            if (k == 2) { PIN(); __builtin_amdgcn_raw_buffer_store_b128(xw, ws, kmoff, mb, SC1); PIN(); }
            if (k == 3 && SPEC_AT == 0) ask_rows1(gm[1], ws, gmo1, gsrc);
            if (k == 0) mfv0(s0, KX[0][0], X[0]); else mfv(s0, KX[k][0], X[k]);
            if (SPEC_AT == 0) { if (k == 2) ask_rows0(gm[0], ws, gmo0, gsrc); else if (k == 3) ask_rows2(gm[2], ws, gmo2, gsrc); }
            if (k == 0) mfv0(s1, KX[0][1], X[0]); else mfv(s1, KX[k][1], X[k]);
        }
        asm volatile("" ::"v"(xw));
        PAD(6); asm volatile("" : "+v"(s0), "+v"(s1), "+v"(sm));
        constexpr float C = 0.08838834764831845f * 1.4426950408889634f;
        float x[10] = {s0[0], s0[1], s0[2], s0[3], s1[0], s1[1], s1[2], s1[3], sm[0], sm[1]};
        float m;
        { float u0, u1;
          asm volatile("s_nop 0\n\tv_max3_f32 %0, %11, %12, %3\n\tv_max3_f32 %1, %4, %5, %6\n\tv_max3_f32 %2, %7, %8, %9\n\tv_max3_f32 %0, %0, %1, %10\n\tv_max_f32 %0, %0, %2\n\t"
              "v_mov_b32 %1, %0\n\ts_nop 1\n\tv_permlane16_swap_b32 %0, %1\n\tv_max_f32 %0, %0, %1\n\tv_mov_b32 %1, %0\n\ts_nop 1\n\tv_permlane32_swap_b32 %0, %1\n\t"
              "v_max_f32 %0, %0, %1\n\tv_mul_f32 %0, %13, %0"
              : "=&v"(m), "=&v"(u0), "=&v"(u1) : "v"(x[0]), "v"(x[1]), "v"(x[2]), "v"(x[3]), "v"(x[4]), "v"(x[5]), "v"(x[6]), "v"(x[7]), "v"(x[8]), "v"(x[9]), "s"(C)); }
#pragma unroll
        for (int i = 0; i < 10; ++i) x[i] = __builtin_amdgcn_exp2f(x[i] * C - m);
        Pm.x = pk2(x[8], x[9]);
        v4u Px = {pk2(x[0], x[1]), pk2(x[2], x[3]), pk2(x[4], x[5]), pk2(x[6], x[7])};
        f32x4 o0, o1, ls;
        asm volatile("s_nop 1" : "+v"(Px), "+v"(Pm));
        mfv0(ls, ONE1, Px);
        if (SPEC_AT == 1) ask_rows0(gm[0], ws, gmo0, gsrc);
        mfv(ls, ONE1, Pm);
        if (SPEC_AT == 1) ask_rows1(gm[1], ws, gmo1, gsrc);
        mfv0(o0, pv[0], Px);
        if (SPEC_AT == 1) ask_rows2(gm[2], ws, gmo2, gsrc);
        mfv0(o1, pv[1], Px); mfv(o0, v0, Pm); mfv(o1, v1, Pm);
        PAD(3); asm volatile("" : "+v"(ls));
        float inv = __builtin_amdgcn_rcpf(ls[0]);
        PAD(2); asm volatile("" : "+v"(o0), "+v"(o1), "+v"(inv));
        o0 *= inv; o1 *= inv;
        st16((v4u){pk2(o0[0], o0[1]), pk2(o0[2], o0[3]), pk2(o1[0], o1[1]), pk2(o1[2], o1[3])}, ws, omoff, omb);
        PIN();
        asm volatile("s_memrealtime s[96:97]" : "={s[96:97]}"(stored)::"memory");
        STAMPA(it, 2); SACC_AT(5);
        cfence();
        CLEAR_ACC();
        lds16w(blk, pk[0]); lds16w(blk + 1024, pk[1]);
        if (__builtin_expect(it == 2 * NSEG - 1, 0)) break;
        rows_check(ws, gmo0, gmo1, gmo2, gsrc, from, gm[0], gm[1], gm[2]);
        before = (int)now;
        if (__builtin_expect(late != 0, 0)) {
            wait_flags<0x602>(ws, fk0 + fsrc, ns);
#pragma unroll
            for (int i = 0; i < 2; ++i) pk[i] = ld16(ws, kvo + i * 1024, pfb);
#pragma unroll
            for (int i = 0; i < 2; ++i) lds16w(blk + i * 1024, pk[i]);
        }
        ++it;
        if (__builtin_expect(it == 64, 0)) tables(1);
    }
    STAMP_DUMP(); SACC_DUMP();
}

DEV void warm_weights(const Params& p, int id, int n, int w, int lane) {
    const int per = (5120 + 4 * n - 1) / (4 * n);
#pragma unroll
    for (int m = 0; m < WARM; ++m) {
        const unsigned char* M = (const unsigned char*)(m == 0 ? p.Wq : m == 1 ? p.Wk : m == 2 ? p.Wv : p.Wo);
        for (int i = 0; i < per; ++i) glds16(M + (size_t)min((id * 4 + w) * per + i, 5119) * 1024 + lane * 16, (u32)(w * 1024));
    }
    drain();
    bar();
}

__global__ void __launch_bounds__(256, 1) mega(Params p) {
    extern __shared__ __attribute__((aligned(16))) unsigned char smem[];
    const int lane = threadIdx.x & 63, w = __builtin_amdgcn_readfirstlane(threadIdx.x >> 6);
    const int x = blockIdx.x & 7, slot = blockIdx.x >> 3;
    const rsrc_t ws = __builtin_amdgcn_make_buffer_rsrc(p.ws, 0, WS_USED, 0x00020000);
    const bool chain = x < 2 ? slot < 16 : slot < 20;
    const int oth = x < 2 ? x * 16 + slot - 16 : 32 + (x - 2) * 12 + slot - 20;
    if (x < 4 && !chain && oth == 40) probe_pages(p, ws, w, lane);
    {
        const int hid = x < 4 ? ((chain || (oth < 32 && (oth & 8))) ? -1 : oth < 32 ? (oth >> 4) * 8 + (oth & 7) : oth - 16)
                      : (slot >= 16 && blockIdx.x != 31 * 8 + 4) ? 40 + (7 - x) * 16 + slot - 16 : -1;
        if (hid >= 0) warm_weights(p, hid, 103, w, lane);
    }
    if (x < 4) {
        if (chain) {
            if (x < 2) role_a(p, ws, x * 16 + slot, x % COPIES, w, lane); else role_b(p, ws, x - 2, slot, w, lane);
        } else if (oth < 32) {
            if (oth & 8) { norm_input(p, ws, oth >> 4, oth & 7, w, lane); role_rows<0>(p, ws, oth >> 4, oth & 7, w, lane); } else role_att(p, ws, oth >> 4, (oth & 7) * 4 + w, w, lane);
        } else {
            const int l = oth >= 44, j = oth - 32 - 12 * l;
            if (j < 8) role_gemm<1>(p, ws, l, 0, j, w, lane); else if (l == 0) role_rows<1>(p, ws, 0, j - 8, w, lane); else role_rows<2>(p, ws, 1, j - 8, w, lane);
        }
    } else {
        const int y = x - 4;
        if (slot < 16 || (slot == 31 && y == 0)) {
            const int id = slot < 16 ? y * 16 + slot : 64;
            if (id < 43) role_qkv<false>(p, ws, id, w, lane); else role_qkv<true>(p, ws, id - 43, w, lane);
        } else if (slot < 26) { const int grp = 2 * y + (slot - 16) / 5; role_gemm<0>(p, ws, grp >> 2, grp & 3, (slot - 16) % 5, w, lane); }
        else if (slot < 31) role_gemm<2>(p, ws, y >> 1, y & 1, slot - 26, w, lane);
        else if (y < 3) role_gemm<1>(p, ws, y - 1, 0, 8, w, lane);
    }
    CLK_EXIT();
}

extern "C" void kernel_launch(void* const* d_in, const int* in_sizes, int n_in, void* d_out, int out_size, void* d_ws, size_t ws_size, hipStream_t stream) {
    Params p;
    p.x = (const u16*)d_in[0]; p.mem0 = (const u16*)d_in[1]; p.Wq = (const u16*)d_in[2]; p.Wk = (const u16*)d_in[3]; p.Wv = (const u16*)d_in[4];
    p.Wo = (const u16*)d_in[5]; p.w1 = (const u16*)d_in[8]; p.w2 = (const u16*)d_in[9];
    p.out = (u16*)d_out; p.ws = (unsigned char*)(((uintptr_t)d_ws + (2u << 20) - 1) & ~(uintptr_t)((2u << 20) - 1));
    hipFuncSetAttribute((const void*)mega, hipFuncAttributeMaxDynamicSharedMemorySize, LDS_BYTES);
    hipMemsetAsync(p.ws, 0xff, CLEAR_BYTES, stream);
    hipLaunchKernelGGL(mega, dim3(256), dim3(256), LDS_BYTES, stream, p);
}
```
